```python
import torch

def _softmax(x, axis=-1):
    return torch.softmax(x, dim=axis)
def _gelu(x):
    return torch.nn.functional.gelu(x, approximate="tanh")
def _silu(x):
    return torch.nn.functional.silu(x)
def _sigmoid(x):
    return torch.sigmoid(x)
def _layernorm(x, axis=-1, eps=1e-5):
    m = x.mean(dim=axis, keepdim=True)
    v = x.var(dim=axis, keepdim=True, unbiased=False)
    return (x - m) / torch.sqrt(v + eps)
def _scatter_add(tgt, idx, src, axis=0):
    return tgt.clone().index_add_(axis, idx, src)
def _segment_sum(src, seg, n):
    out = torch.zeros((n,) + src.shape[1:], dtype=src.dtype)
    return out.index_add_(0, seg, src)
def _pad(x, pw):
    flat = tuple(v for pair in reversed(pw) for v in pair)
    return torch.nn.functional.pad(x, flat)

kernel_name = 'recurrent_scan__sliding_local_dilated__glu_gated_bilinear__prompt'

def setup_inputs():

    _w = (lambda a: (lambda t: t.to(torch.bfloat16) if t.is_floating_point() else t)(torch.as_tensor(a)))
    def _A_setup():
        B, S, D = 1, 2048, 448
        return {
            "x": _w((lambda *s: torch.randn(*s, dtype=torch.bfloat16) * 0.05)(B, S, D)),
            "wqkv": _w((lambda *s: torch.randn(*s, dtype=torch.bfloat16) * 0.2)(D, 3 * D)),
            "wo": _w((lambda *s: torch.randn(*s, dtype=torch.bfloat16) * 0.2)(D, D)),
        }
    _A_in = _A_setup()

    def _B_setup():
        B, S, D, F = 1, 2048, 448, 1792
        return {
            "x": _w((lambda *s: torch.randn(*s, dtype=torch.bfloat16) * 0.05)(B, S, D)),
            "w_gate": _w((lambda *s: torch.randn(*s, dtype=torch.bfloat16) * 0.1)(D, F) * (D ** -0.5)),
            "w_up": _w((lambda *s: torch.randn(*s, dtype=torch.bfloat16) * 0.1)(D, F) * (D ** -0.5)),
            "w_down": _w((lambda *s: torch.randn(*s, dtype=torch.bfloat16) * 0.1)(F, D) * (F ** -0.5)),
        }
    _B_in = _B_setup()

    return {
        "A_x": _A_in["x"],
        "A_wqkv": _A_in["wqkv"],
        "A_wo": _A_in["wo"],
        "B_x": _B_in["x"],
        "B_w_gate": _B_in["w_gate"],
        "B_w_up": _B_in["w_up"],
        "B_w_down": _B_in["w_down"],
    }

def reference(A_x, A_wqkv, A_wo, B_x, B_w_gate, B_w_up, B_w_down):
    def _A_ref(x, wqkv, wo):
        B, S, D = x.shape
        H = 16; Dh = D // H
        W, dil = 8, 2
        qkv = (lambda x, *d: x.permute(*d))((x @ wqkv).reshape(B, S, 3, H, Dh), 2, 0, 3, 1, 4)
        q, k, v = qkv[0], qkv[1], qkv[2]
        scale = Dh ** -0.5
        pos = torch.arange(S)
        off = torch.arange(W) * dil
        idx = pos[:, None] - off[None, :]
        mask = idx >= 0
        idx_c = torch.where(mask, idx, 0).reshape(-1)
        kg = k[:, :, idx_c, :].reshape(B, H, S, W, Dh)
        vg = v[:, :, idx_c, :].reshape(B, H, S, W, Dh)
        logits = torch.einsum("bhsd,bhswd->bhsw", q, kg) * scale
        logits = torch.where(mask[None, None, :, :], logits, -1e9)
        attn = _softmax(logits, axis=-1)
        o = torch.einsum("bhsw,bhswd->bhsd", attn, vg)
        return (lambda x, *d: x.permute(*d))(o, 0, 2, 1, 3).reshape(B, S, D) @ wo

    def _B_ref(x, w_gate, w_up, w_down):
        g = torch.einsum("bsd,df->bsf", x, w_gate)
        u = torch.einsum("bsd,df->bsf", x, w_up)
        h = g * u
        return torch.einsum("bsf,fd->bsd", h, w_down)

    x = A_x
    xn = _layernorm(x, axis=-1)
    s = _A_ref(xn, A_wqkv, A_wo)
    g = _silu(_B_ref(xn, B_w_gate, B_w_up, B_w_down))
    return x + s * g
```

```cpp
#include <hip/hip_runtime.h>
#include <hip/hip_bf16.h>
typedef unsigned short u16;
__device__ __forceinline__ float bf2f(u16 x){ return __uint_as_float(((unsigned)x)<<16); }
__device__ __forceinline__ u16 f2bf(float f){ unsigned u=__float_as_uint(f); u += 0x7fffu + ((u>>16)&1u); return (u16)(u>>16); }
__device__ __forceinline__ float rbf(float f){ return bf2f(f2bf(f)); }
constexpr int S=2048, D=448, F=1792, H=16, DH=28;

__global__ void ln_k(const u16* x, u16* xn){
  int row=blockIdx.x; int lane=threadIdx.x;
  float v[7]; float s=0;
  for(int i=0;i<7;i++){ v[i]=bf2f(x[row*D+lane+64*i]); s+=v[i]; }
  for(int o=32;o;o>>=1) s+=__shfl_xor(s,o);
  float mean=s/D; float q=0;
  for(int i=0;i<7;i++){ float d=v[i]-mean; q+=d*d; }
  for(int o=32;o;o>>=1) q+=__shfl_xor(q,o);
  float var=rbf(q/D); float m=rbf(mean);
  float den=rbf(sqrtf(rbf(var+1e-5f)));
  for(int i=0;i<7;i++){ xn[row*D+lane+64*i]=f2bf(rbf(v[i]-m)/den); }
}
__global__ void gemm_k(const u16* A,const u16* B,u16* C,int M,int N,int K){
  int n=blockIdx.x*64+threadIdx.x; int m=blockIdx.y*4+threadIdx.y;
  if(n>=N) return;
  float acc=0;
  for(int k=0;k<K;k++) acc+=bf2f(A[m*K+k])*bf2f(B[k*N+n]);
  C[m*N+n]=f2bf(acc);
}
__global__ void attn_k(const u16* qkv,u16* o){
  int idx=blockIdx.x*blockDim.x+threadIdx.x; int s=idx/H, h=idx%H;
  const u16* q=qkv+s*3*D+h*DH;
  float lg[8]; float mx=-1e30f;
  const float scale=0.18898223650461362f;
  for(int j=0;j<8;j++){ int t=s-2*j; float l;
    if(t>=0){ const u16* k=qkv+t*3*D+D+h*DH; float a=0; for(int d=0;d<DH;d++) a+=bf2f(q[d])*bf2f(k[d]); l=rbf(rbf(a)*scale);} else l=rbf(-1e9f);
    lg[j]=l; mx=fmaxf(mx,l);}
  float sum=0; for(int j=0;j<8;j++){ lg[j]=__expf(lg[j]-mx); sum+=lg[j]; }
  for(int j=0;j<8;j++) lg[j]=rbf(lg[j]/sum);
  for(int d=0;d<DH;d++){ float a=0; for(int j=0;j<8;j++){ int t=s-2*j; if(t<0)t=0; a+=lg[j]*bf2f(qkv[t*3*D+2*D+h*DH+d]); } o[s*D+h*DH+d]=f2bf(a);}
}
__global__ void mul_k(const u16* g,const u16* u,u16* h,int n){ int i=blockIdx.x*256+threadIdx.x; if(i<n) h[i]=f2bf(bf2f(g[i])*bf2f(u[i])); }
__global__ void fin_k(const u16* x,const u16* s,const u16* d,u16* out,int n){ int i=blockIdx.x*256+threadIdx.x; if(i<n){ float dd=bf2f(d[i]); float g=rbf(dd/(1.f+__expf(-dd))); out[i]=f2bf(bf2f(x[i])+rbf(bf2f(s[i])*g)); } }

extern "C" void kernel_launch(void* const* d_in, const int* in_sizes, int n_in, void* d_out, int out_size, void* d_ws, size_t ws_size, hipStream_t stream){
  const u16* x=(const u16*)d_in[0]; const u16* wqkv=(const u16*)d_in[1]; const u16* wo=(const u16*)d_in[2];
  const u16* wg=(const u16*)d_in[4]; const u16* wu=(const u16*)d_in[5]; const u16* wd=(const u16*)d_in[6];
  u16* ws=(u16*)d_ws;
  u16* xn=ws; u16* qkv=xn+S*D; u16* o=qkv+S*3*D; u16* s=o+S*D; u16* g=s+S*D; u16* u=g+S*F; u16* h=u+S*F; u16* d=h+S*F;
  ln_k<<<S,64,0,stream>>>(x,xn);
  gemm_k<<<dim3(3*D/64,S/4),dim3(64,4),0,stream>>>(xn,wqkv,qkv,S,3*D,D);
  attn_k<<<S*H/256,256,0,stream>>>(qkv,o);
  gemm_k<<<dim3(D/64,S/4),dim3(64,4),0,stream>>>(o,wo,s,S,D,D);
  gemm_k<<<dim3(F/64,S/4),dim3(64,4),0,stream>>>(xn,wg,g,S,F,D);
  gemm_k<<<dim3(F/64,S/4),dim3(64,4),0,stream>>>(xn,wu,u,S,F,D);
  mul_k<<<S*F/256,256,0,stream>>>(g,u,h,S*F);
  gemm_k<<<dim3(D/64,S/4),dim3(64,4),0,stream>>>(h,wd,d,S,D,F);
  fin_k<<<S*D/256,256,0,stream>>>(x,s,d,(u16*)d_out,S*D);
}
```

```cpp
#include <hip/hip_runtime.h>
#pragma clang diagnostic ignored "-Wint-to-pointer-cast"
typedef unsigned u32; typedef unsigned long long u64;
typedef short bf16x8 __attribute__((ext_vector_type(8)));
typedef short s16x4 __attribute__((ext_vector_type(4)));
typedef __bf16 bf2_t __attribute__((ext_vector_type(2)));
typedef float f32x2 __attribute__((ext_vector_type(2)));
typedef float f32x4 __attribute__((ext_vector_type(4)));
typedef float f32x16 __attribute__((ext_vector_type(16)));
typedef unsigned long long u64x8 __attribute__((ext_vector_type(8)));
typedef unsigned u32x2 __attribute__((ext_vector_type(2)));
typedef unsigned u32x4 __attribute__((ext_vector_type(4)));
#define LAS __attribute__((address_space(3)))
#define DI __device__ __forceinline__
#define PW "7"
#define CODEPF 220
#define WOBASE 8192

constexpr int S_ = 2048, D_ = 448, F_ = 1792;
constexpr u32 WS_EPOCH = 0;
constexpr u32 WS_STAT = 4096;
constexpr u32 WS_CS = WS_STAT + 16384;
constexpr u32 WS_FO = 65536;
constexpr u32 WS_FL = WS_FO + 256 * 128;
constexpr u32 WS_K = 327680;
constexpr u32 WS_V = WS_K + 64 * 28672;
constexpr u32 WS_O = WS_V + 64 * 32768;
constexpr u32 WS_H = WS_O + 64 * 32768;
constexpr u32 WS_END = WS_H + 64 * 114688;
constexpr int SPIN_CAP = 32767;

#define WIN(A, N) do { for (int k_ = 1; k_ < N; ++k_) { A[k_] = A[0] + 65536 * k_; asm volatile("" : "+v"(A[k_])); } } while (0)
#define SCHED() __builtin_amdgcn_sched_barrier(0)
#define GLDS(LOFF, VOFF, RS, SOFF) asm volatile("s_add_u32 m0, %0, %1\n\ts_nop 0\n\tbuffer_load_dwordx4 %2, %3, %4 offen lds" \
    :: "s"(m0w), "i"(LOFF), "v"(VOFF), "s"(RS), "s"((u32)(SOFF)) : "memory", "scc")
#define GLDS3(LOFF, VOFF, RS, SB_, SI) do { u32 so_; asm volatile("s_add_u32 m0, %1, %2\n\ts_add_u32 %0, %5, %6\n\tbuffer_load_dwordx4 %3, %4, %0 offen lds" \
    : "=&s"(so_) : "s"(m0w), "i"(LOFF), "v"(VOFF), "s"(RS), "s"((u32)(SB_)), "i"(SI) : "memory", "scc"); } while (0)
#define BLOAD4(DST, VOFF, RS, SOFF, IMM) asm volatile("buffer_load_dwordx4 %0, %1, %2, %3 offen offset:%4 sc1" \
    : "=v"(DST) : "v"(VOFF), "s"(RS), "s"((u32)(SOFF)), "i"(IMM) : "memory")
#define BLOAD4P(DST, VOFF, RS, SOFF, IMM) asm volatile("buffer_load_dwordx4 %0, %1, %2, %3 offen offset:%4" \
    : "=v"(DST) : "v"(VOFF), "s"(RS), "s"((u32)(SOFF)), "i"(IMM) : "memory")
#define ST4(V, VOFF) __builtin_amdgcn_raw_buffer_store_b128(V, rWS, (int)(VOFF), 0, 16)
#define ST2(V, VOFF) __builtin_amdgcn_raw_buffer_store_b64(V, rWS, (int)(VOFF), 0, 16)
#define BLOAD1(DST, VOFF) asm volatile("buffer_load_dword %0, %1, %2, 0 offen sc1" : "=v"(DST) : "v"(VOFF), "s"(rWS) : "memory")
#define FLAG(SOFF) asm volatile("s_mov_b64 exec, 1\n\tbuffer_store_dword %0, off, %1, %2 sc1\n\ts_mov_b64 exec, -1" :: "v"(vep), "s"(rWS), "s"((u32)(SOFF)) : "memory")
#define FLAG_W(SOFF) asm volatile("s_cmp_lg_u32 %3, 0\n\ts_cbranch_scc1 3f\n\ts_mov_b64 exec, 1\n\tbuffer_store_dword %0, off, %1, %2 sc1\n\ts_mov_b64 exec, -1\n3:" \
    :: "v"(vep), "s"(rWS), "s"((u32)(SOFF)), "s"(w) : "memory", "scc")
#define POLL_W(PV, VOFF) asm volatile("s_cmp_lg_u32 %3, " PW "\n\ts_cbranch_scc1 3f\n\tbuffer_load_dword %0, %1, %2, 0 offen sc1\n3:" \
    : "=&v"(PV) : "v"(VOFF), "s"(rWS), "s"(w) : "memory", "scc")
#define CHECK_W(PV, VOFF, N) do { u32 c_; asm volatile( \
    "s_cmp_lg_u32 %6, " PW "\n\ts_cbranch_scc1 2f\n\ts_waitcnt vmcnt(%5)\n\tv_cmp_ne_u32 vcc, %3, %0\n\ts_cbranch_vccz 2f\n\ts_movk_i32 %1, 0x7fff\n" \
    "1:\n\tbuffer_load_dword %0, %2, %4, 0 offen sc1\n\ts_waitcnt vmcnt(0)\n\tv_cmp_ne_u32 vcc, %3, %0\n\ts_cbranch_vccz 2f\n\t" \
    "s_sleep 3\n\ts_sub_u32 %1, %1, 1\n\ts_cbranch_scc0 1b\n2:" \
    : "+v"(PV), "=&s"(c_) : "v"(VOFF), "s"(ep), "s"(rWS), "i"(N), "s"(w) : "memory", "vcc", "scc"); } while (0)
#define VTOUCH(D, WV, VA, RA, VB, RB) asm volatile("s_cmp_lg_u32 %5, %6\n\ts_cbranch_scc1 3f\n\tbuffer_load_dword %0, %1, %2, 0 offen\n\tbuffer_load_dword %0, %3, %4, 0 offen\n3:" \
    : "=&v"(D) : "v"(VA), "s"(RA), "v"(VB), "s"(RB), "s"(w), "i"(WV) : "memory", "scc")
#define EPOCH_ISSUE() u32 e0; asm volatile("s_load_dword %0, %1, 0x0" : "=s"(e0) : "s"(ws) : "memory")
#define EPOCH_LOAD() do { asm volatile("s_waitcnt lgkmcnt(0)\n\ts_add_u32 %0, %1, 0x9E3779B9" : "=s"(ep) : "s"(e0) : "memory", "scc"); vep = ep; asm volatile("" : "+v"(vep)); } while (0)
#define POLL_CHECK(PV, VOFF, N) do { u32 c_; asm volatile( \
    "s_waitcnt vmcnt(%5)\n\tv_cmp_ne_u32 vcc, %3, %0\n\ts_cbranch_vccz 2f\n\ts_movk_i32 %1, 0x7fff\n" \
    "1:\n\tbuffer_load_dword %0, %2, %4, 0 offen sc1\n\ts_waitcnt vmcnt(0)\n\tv_cmp_ne_u32 vcc, %3, %0\n\ts_cbranch_vccz 2f\n\t" \
    "s_sleep 3\n\ts_sub_u32 %1, %1, 1\n\ts_cbranch_scc0 1b\n2:" \
    : "+v"(PV), "=&s"(c_) : "v"(VOFF), "s"(ep), "s"(rWS), "i"(N) : "memory", "vcc", "scc"); } while (0)
#define MA32(ACC, A, B) asm volatile("v_mfma_f32_32x32x16_bf16 %0, %1, %2, %0" : "+v"(ACC) : "v"(A), "v"(B))
#define MZ32(ACC, A, B) asm volatile("v_mfma_f32_32x32x16_bf16 %0, %1, %2, 0" : "=&v"(ACC) : "v"(A), "v"(B))
#define MA16(ACC, A, B) asm volatile("v_mfma_f32_16x16x32_bf16 %0, %1, %2, %0" : "+v"(ACC) : "v"(A), "v"(B))
#define MZ16(ACC, A, B) asm volatile("v_mfma_f32_16x16x32_bf16 %0, %1, %2, 0" : "=&v"(ACC) : "v"(A), "v"(B))

DI u32x4 LDS128(u32 a) { return *(const LAS u32x4*)a; }
DI u32x4 TRF(u32 a, u32 d) {
  s16x4 lo = __builtin_amdgcn_ds_read_tr16_b64_v4i16((LAS s16x4*)a);
  s16x4 hi = __builtin_amdgcn_ds_read_tr16_b64_v4i16((LAS s16x4*)(a + d));
  u32x2 l = __builtin_bit_cast(u32x2, lo), h = __builtin_bit_cast(u32x2, hi);
  return u32x4{l.x, l.y, h.x, h.y};
}
DI u32x2 TRH(u32 a) { return __builtin_bit_cast(u32x2, __builtin_amdgcn_ds_read_tr16_b64_v4i16((LAS s16x4*)a)); }
DI u32x4 TRJ(u32x2 l, u32x2 h) { return u32x4{l.x, l.y, h.x, h.y}; }
DI u32x4 TRF2(u32 a, u32 b) {
  s16x4 lo = __builtin_amdgcn_ds_read_tr16_b64_v4i16((LAS s16x4*)a);
  s16x4 hi = __builtin_amdgcn_ds_read_tr16_b64_v4i16((LAS s16x4*)b);
  u32x2 l = __builtin_bit_cast(u32x2, lo), h = __builtin_bit_cast(u32x2, hi);
  return u32x4{l.x, l.y, h.x, h.y};
}
DI f32x16 MFMA32(u32x4 a, u32x4 b, f32x16 c) {
  return __builtin_amdgcn_mfma_f32_32x32x16_bf16(__builtin_bit_cast(bf16x8, a), __builtin_bit_cast(bf16x8, b), c, 0, 0, 0);
}
DI f32x4 MFMA16(u32x4 a, u32x4 b, f32x4 c) {
  return __builtin_amdgcn_mfma_f32_16x16x32_bf16(__builtin_bit_cast(bf16x8, a), __builtin_bit_cast(bf16x8, b), c, 0, 0, 0);
}
DI u32 PK(float a, float b) { f32x2 f = {a, b}; return __builtin_bit_cast(u32, __builtin_convertvector(f, bf2_t)); }
DI float BLO(u32 v) { return __uint_as_float(v << 16); }
DI float BHI(u32 v) { return __uint_as_float(v & 0xffff0000u); }
DI float RBF(float f) { return BLO(PK(f, 0.f) & 0xffffu); }
DI u32x4 CSFRAG(u32 v) {
  u32 hi = v & 0xffff0000u; float r1 = __uint_as_float(v) - __uint_as_float(hi);
  u32 mid = __float_as_uint(r1) & 0xffff0000u; float r2 = r1 - __uint_as_float(mid);
  return u32x4{(hi >> 16) | mid, __float_as_uint(r2) >> 16, 0u, 0u};
}
DI float DOT2(u32 a, u32 b, float c) { return __builtin_amdgcn_fdot2_f32_bf16(__builtin_bit_cast(bf2_t, a), __builtin_bit_cast(bf2_t, b), c, false); }
DI float FMAX(float a, float b) { return __builtin_elementwise_maximum(a, b); }
DI f32x4 MFMA16H(u32x2 a, u32x2 b, f32x4 c) {
  return __builtin_amdgcn_mfma_f32_16x16x16bf16_1k(__builtin_bit_cast(s16x4, a), __builtin_bit_cast(s16x4, b), c, 0, 0, 0);
}
DI float sw16_max(float a, float b) { auto r = __builtin_amdgcn_permlane16_swap(__float_as_uint(a), __float_as_uint(b), false, false); return FMAX(__uint_as_float(r[0]), __uint_as_float(r[1])); }
DI float sw32_max(float a, float b) { auto r = __builtin_amdgcn_permlane32_swap(__float_as_uint(a), __float_as_uint(b), false, false); return FMAX(__uint_as_float(r[0]), __uint_as_float(r[1])); }
DI void bcast4(float z, float (&o)[4]) {
  auto r = __builtin_amdgcn_permlane32_swap(__float_as_uint(z), __float_as_uint(z), false, false);
  auto a = __builtin_amdgcn_permlane16_swap(r[0], r[0], false, false);
  auto b = __builtin_amdgcn_permlane16_swap(r[1], r[1], false, false);
  o[0] = __uint_as_float(a[0]); o[1] = __uint_as_float(a[1]); o[2] = __uint_as_float(b[0]); o[3] = __uint_as_float(b[1]);
}
DI float sw16_add(float a, float b) { auto r = __builtin_amdgcn_permlane16_swap(__float_as_uint(a), __float_as_uint(b), false, false); return __uint_as_float(r[0]) + __uint_as_float(r[1]); }
DI float sw32_add(float a, float b) { auto r = __builtin_amdgcn_permlane32_swap(__float_as_uint(a), __float_as_uint(b), false, false); return __uint_as_float(r[0]) + __uint_as_float(r[1]); }
template <int CTRL> DI float dpp(float x) { return __uint_as_float(__builtin_amdgcn_update_dpp(0u, __float_as_uint(x), CTRL, 0xf, 0xf, true)); }
DI float red8_add(float x) { x += dpp<0xB1>(x); x += dpp<0x4E>(x); x += dpp<0x141>(x); return x; }

#define KA(s) (((s) + arot >= 7) ? (s) + arot - 7 : (s) + arot)

__global__ __launch_bounds__(512, 2) void fused_kernel(const void*, const void*, const void*, const void*, const void*, const void*, void*, void*) {
  __shared__ __attribute__((aligned(1024))) char lds[163840];
  u64x8 ka;
  asm volatile("s_load_dwordx16 %0, %1, 0x0" : "=s"(ka) : "s"(__builtin_amdgcn_kernarg_segment_ptr()) : "memory");
  const u32 lb = (u32)(size_t)(LAS char*)lds;
  const int tid = threadIdx.x, lane = tid & 63;
  const u32 w = __builtin_amdgcn_readfirstlane(tid >> 6);
  const u32 xcd = blockIdx.x & 7, li = blockIdx.x >> 3, half = xcd >> 2, quad = xcd & 3;
  const u32 m0w = lb + w * 1024;
#define KARGS() asm volatile("s_waitcnt lgkmcnt(0)" : "+s"(ka) :: "memory"); \
  void *x = (void*)ka[0], *wqkv = (void*)ka[1], *wo = (void*)ka[2], *wg = (void*)ka[3], *wu = (void*)ka[4], *wd = (void*)ka[5], *out = (void*)ka[6]; ws = (void*)ka[7]; \
  const __amdgpu_buffer_rsrc_t rX = __builtin_amdgcn_make_buffer_rsrc(x, 0, S_ * D_ * 2, 0x00020000); \
  const __amdgpu_buffer_rsrc_t rQKV = __builtin_amdgcn_make_buffer_rsrc(wqkv, 0, D_ * 3 * D_ * 2, 0x00020000); \
  const __amdgpu_buffer_rsrc_t rWS = __builtin_amdgcn_make_buffer_rsrc(ws, 0, WS_END, 0x00020000)
  void* ws;
  u32 ep, vep;
  const u32 r8 = 8 * w + (lane >> 3), c8 = lane & 7;
  u32 vx = (32 * (r8 >> 5) + 2 * (r8 & 15) + ((r8 >> 4) & 1)) * 896 + ((c8 ^ ((r8 >> 1) & 7)) << 4);
  {
    u64 pc; asm volatile("s_getpc_b64 %0" : "=s"(pc));
    if (w == 7 && lane < 8 && li * 8 + lane < CODEPF) { const u64 a_ = (pc & ~127ull) + (li * 8 + lane) * 128;
      asm volatile("s_add_u32 m0, %0, %1\n\ts_nop 0\n\tglobal_load_lds_dword %2, off" :: "s"(m0w), "i"(19 * 8192), "v"(a_) : "memory", "scc"); }
  }
  const u32 T0 = 28 * quad + li - 4;
  u32 vfirst = li >= 4 ? r8 * 2688 + ((c8 ^ ((((r8 >> 1) & 1) | (((r8 >> 3) & 1) << 1)) << 1)) << 4) : r8 * 896 + c8 * 16;
  u32 sfirst = (li >= 4 ? 128 * (8 * half + (T0 & 7)) : 128 * (8 * half + 4 * (quad >> 1) + li) + 64 * (quad & 1)) * 896, skv0 = (448 + 64 * (T0 >> 3)) * 2;
  asm volatile("" : "+v"(vx), "+v"(vfirst));
  asm volatile("" : "+s"(sfirst), "+s"(skv0));
  KARGS();
  if (__builtin_expect(li >= 4, 1)) {
    const u32 f = li - 4, T = 28 * quad + f, j = T >> 3, g = 8 * half + (T & 7);
    const u32 fq = f / 7, ct7 = f - 7 * fq, rt2 = 4 * xcd + fq;
    const u32 csw = (c8 ^ ((((r8 >> 1) & 1) | (((r8 >> 3) & 1) << 1)) << 1)) << 4, csw2 = (c8 ^ (((r8 >> 1) & 3) << 1)) << 4;
    const u32 vwq = vfirst, sx = sfirst, skv = skv0, vwg = r8 * 3584 + csw, vw2 = r8 * 896 + csw2, sgu = 256 * j, sw2 = ct7 * 128;
    const __amdgpu_buffer_rsrc_t rG = __builtin_amdgcn_make_buffer_rsrc((void*)wg, 0, D_ * F_ * 2, 0x00020000);
    const __amdgpu_buffer_rsrc_t rU = __builtin_amdgcn_make_buffer_rsrc((void*)wu, 0, D_ * F_ * 2, 0x00020000);
    const __amdgpu_buffer_rsrc_t rWD = __builtin_amdgcn_make_buffer_rsrc((void*)wd, 0, D_ * F_ * 2, 0x00020000);
    const __amdgpu_buffer_rsrc_t rWO = __builtin_amdgcn_make_buffer_rsrc((void*)wo, 0, D_ * D_ * 2, 0x00020000);
    const __amdgpu_buffer_rsrc_t rOut = __builtin_amdgcn_make_buffer_rsrc(out, 0, S_ * D_ * 2, 0x00020000);
    const u32 rb = w & 3, ch = w >> 2, i16 = lane & 15, qd = lane >> 4;
    const u32 blk = 4 * g + rb;
    u32x4 xf[28], wk0, wk1, wf0[2], wf1[2], wf2[2], hf[28], of[8], xres, wo_[8][2], gr, hp[2], pd[4], mfr[2], cq_[2][2], sel0, sel1;
    f32x4 rd[2], rs[2], a0[2][2], aa[2][2][2], ab[2][2][2], ad[4][2], as[4][2];
    u32x2 kp[2][2], idf, trlo;
    u32 pl, pl3;
    float rinv[2], rinv2[2], dsl[8];
    u32 ax0[3], ax1[3], aw0[3], aw1[3], vgr0, vgr1, vp1, vp3, vh, vxr, aw20[3], aw21[3], aw22[3], aw23[3], vkv, vkv1, vhs, awo[4], apw, apr;
    const u32 b2 = w >> 2, kq = w & 3, blk2 = 2 * rt2 + b2;
    const u32 sfk = WS_FL + (g * 14 + j) * 128, sh = WS_H + blk2 * 57344 + kq * 2048, so = WS_O + blk2 * 32768 + kq * 8192;
    u32 tw2, tw1, tw3;
#define TOUCH_W2(WV) do { const u32 n_ = blockIdx.x - 32; VTOUCH(tw2, WV, lane < 56 ? (n_ * 56 + lane) * 128 : 0x7ffffff0u, rWD, lane < 14 ? (n_ * 14 + lane) * 128 : 0x7ffffff0u, rWO); } while (0)
#define TOUCH_W1(WV) do { const u32 n_ = blockIdx.x - 32, o_ = lane < 28 ? (n_ * 28 + lane) * 256 + 128 : 0x7ffffff0u; VTOUCH(tw1, WV, o_, rG, o_, rU); } while (0)
#define TOUCH_WQ(WV) do { const u32 n_ = blockIdx.x - 32, o_ = lane < 7 ? n_ * 5376 + lane * 128 : 0x7ffffff0u; VTOUCH(tw3, WV, o_, rQKV, o_ + 2688, rQKV); } while (0)
#define FFN_SETUP() do { \
      ax0[0] = lb + (32 * rb + i16) * 128 + ((qd ^ ((i16 >> 1) & 7)) << 4); ax1[0] = ax0[0] ^ 64; WIN(ax0, 3); WIN(ax1, 3); \
      const u32 q_ = i16 >> 2, p_ = lane & 3; \
      aw0[0] = lb + (8 * qd + q_) * 128 + ((64 * ch + 8 * p_) ^ ((((q_ >> 1) & 1) | ((qd & 1) << 1)) << 5)); aw1[0] = aw0[0] ^ 32; WIN(aw0, 3); WIN(aw1, 3); \
      vh = lane * 16; \
      vgr1 = WS_CS + (3 * j + 1) * 1024 + (32 * ch + (lane & 31)) * 16; vgr0 = qd >= 2 ? WS_STAT + blk * 256 + i16 * 16 : vgr1 - 1024; \
      vhs = WS_H + (blk * 56 + 4 * j + 2 * ch) * 1024 + vh; \
      { const u32 c_ = 64 * (j - 7) + 32 * ch + i16, h0 = c_ / 28, h1 = (c_ + 16) / 28, vb = WS_V + blk * 32768 + qd * 8192; \
        vkv = j < 7 ? WS_K + (blk * 112 + 16 * j + 8 * ch + qd) * 256 + i16 * 16 : vb + (c_ + 4 * h0) * 16; \
        vkv1 = j < 7 ? vkv + 1024 : vb + (c_ + 16 + 4 * h1) * 16; \
        asm volatile("" : "+v"(vkv), "+v"(vkv1), "+v"(vgr0), "+v"(vgr1), "+v"(vhs), "+v"(vh)); } \
    } while (0)
#define GRAN_CHECK(VG, IMM, N) do { \
      asm volatile("s_waitcnt vmcnt(%1)" : "+v"(gr) : "i"(N) : "memory"); \
      if (__builtin_expect(__builtin_amdgcn_ballot_w64(gr.y != ep || gr.w != ep) != 0, 0)) { \
        _Pragma("clang loop unroll(disable)") for (int it_ = 0; it_ < SPIN_CAP; ++it_) { \
          __builtin_amdgcn_s_sleep(2); \
          asm volatile("buffer_load_dwordx4 %0, %1, %2, 0 offen offset:%3 sc1\n\ts_waitcnt vmcnt(0)" : "=&v"(gr) : "v"(VG), "s"(rWS), "i"(IMM) : "memory"); \
          if (__builtin_amdgcn_ballot_w64(gr.y != ep || gr.w != ep) == 0) break; } } \
    } while (0)
#define HP2(A, NT, PA, E) PK(A[0][NT][PA][E] * rinv2[PA] * A[1][NT][PA][E], A[0][NT][PA][(E) + 1] * rinv2[PA] * A[1][NT][PA][(E) + 1])
#define HPACK(A, PA) u32x4{HP2(A, 0, PA, 0), HP2(A, 0, PA, 2), HP2(A, 1, PA, 0), HP2(A, 1, PA, 2)}
#define FFN_IDENT() do { const u32 one_ = (i16 >> 2) != qd ? 0u : ((i16 & 1) ? 0x3f800000u : 0x3f80u); \
      idf = u32x2{(i16 & 2) ? 0u : one_, (i16 & 2) ? one_ : 0u}; asm volatile("" : "+v"(idf)); } while (0)
#define CSF2(U, V) do { const u32x4 f_ = CSFRAG(V); \
      cq_[U][0] = u32x4{qd == 0 ? f_.x : 0u, qd == 0 ? f_.y : 0u, 0u, 0u}; cq_[U][1] = u32x4{qd == 1 ? f_.x : 0u, qd == 1 ? f_.y : 0u, 0u, 0u}; } while (0)
#define FFN_K0P() do { \
      CSF2(0, __float_as_uint(__uint_as_float(gr.x) + __uint_as_float(gr.z))); \
      for (int pa = 0; pa < 2; ++pa) { const u32 g_ = pa ? gr.z : gr.x, st_ = __builtin_amdgcn_permlane32_swap(g_, g_, false, false)[1];     \
        const u32 nm = (st_ & 0xffffu) ^ 0x8000u; mfr[pa] = u32x4{nm | (nm << 16), nm, 0u, 0u}; \
        rinv[pa] = __builtin_amdgcn_rcpf(BHI(st_)); rinv2[pa] = rinv[pa] * rinv[pa]; } \
      asm volatile("" : "+v"(cq_[0][0]), "+v"(cq_[0][1]), "+v"(mfr[0]), "+v"(mfr[1]), "+v"(rinv[0]), "+v"(rinv[1]), "+v"(rinv2[0]), "+v"(rinv2[1])); } while (0)
#define CORR(A, U) do { for (int nt = 0; nt < 2; ++nt) for (int pa = 0; pa < 2; ++pa) A[nt][pa] = MFMA16(cq_[U][nt], mfr[pa], A[nt][pa]); \
      asm volatile("" : "+v"(A[0][0]), "+v"(A[0][1]), "+v"(A[1][0]), "+v"(A[1][1])); } while (0)
#define FFN_K0() CORR(a0, 0)
#define FFN_K1() do { const f32x4 z_ = {0.f, 0.f, 0.f, 0.f}; \
      for (int nt = 0; nt < 2; ++nt) for (int pa = 0; pa < 2; ++pa) { \
        kp[nt][pa] = u32x2{PK(a0[nt][pa][0] * rinv[pa], a0[nt][pa][1] * rinv[pa]), PK(a0[nt][pa][2] * rinv[pa], a0[nt][pa][3] * rinv[pa])}; \
        a0[nt][pa] = MFMA16H(kp[nt][pa], idf, z_); }     \
      asm volatile("" : "+v"(a0[0][0]), "+v"(a0[0][1]), "+v"(a0[1][0]), "+v"(a0[1][1])); } while (0)
#define FFN_ST0() do { \
      if (j >= 7) for (int nt = 0; nt < 2; ++nt) for (int pa = 0; pa < 2; ++pa) kp[nt][pa] = u32x2{PK(a0[nt][pa][0], a0[nt][pa][1]), PK(a0[nt][pa][2], a0[nt][pa][3])}; \
      ST4((u32x4{kp[0][0].x, kp[0][0].y, kp[0][1].x, kp[0][1].y}), vkv); ST4((u32x4{kp[1][0].x, kp[1][0].y, kp[1][1].x, kp[1][1].y}), vkv1); \
    } while (0)
#define FFN_CP() do { CSF2(0, gr.x); CSF2(1, gr.z); asm volatile("" : "+v"(cq_[0][0]), "+v"(cq_[0][1]), "+v"(cq_[1][0]), "+v"(cq_[1][1])); } while (0)
#define FFN_A0() do { CORR(aa[0], 0); CORR(aa[1], 1); } while (0)
#define FFN_A1(S_) do { hp[S_] = HPACK(aa, S_); asm volatile("" : "+v"(hp[S_])); } while (0)
#define FFN_STA() do { ST4(hp[0], vhs); ST4(hp[1], vhs + 1024); } while (0)
#define FFN_EPIB() do { CORR(ab[0], 0); CORR(ab[1], 1); \
      hp[0] = HPACK(ab, 0); hp[1] = HPACK(ab, 1); \
      ST4(hp[0], vhs + 64 * 57344); ST4(hp[1], vhs + 64 * 57344 + 1024); \
    } while (0)
#define P2_SETUP() do { \
      const u32 q_ = i16 >> 2, p_ = lane & 3, t2 = lb + (4 * qd + q_) * 128 + ((8 * p_) ^ (((2 * qd + (q_ >> 1)) & 3) << 5)); \
      aw20[0] = t2 + kq * 4096; aw21[0] = aw20[0] ^ 32; aw22[0] = aw20[0] ^ 64; aw23[0] = aw20[0] ^ 96; \
      WIN(aw20, 3); WIN(aw21, 3); WIN(aw22, 3); WIN(aw23, 3); \
      vp1 = WS_FL + ((rt2 >> 1) * 14 + (lane < 14 ? lane : 13)) * 128 + 4; \
      vp3 = WS_FO + ((rt2 >> 1) * 16 + 4 * kq + (lane >> 4)) * 128; \
      { const u32 R = tid >> 3, ii = R & 31; vxr = (64 * rt2 + (R & 32) + 2 * (ii & 15) + (ii >> 4)) * 896 + ct7 * 128 + c8 * 16; } \
      for (int c = 0; c < 4; ++c) awo[c] = (t2 ^ (32 * c)) + WOBASE + 112 * kq * 128; \
      asm volatile("" : "+v"(vp1), "+v"(vp3), "+v"(vxr), "+v"(awo[0]), "+v"(awo[1]), "+v"(awo[2]), "+v"(awo[3]), "+v"(aw20[0]), "+v"(aw21[0]), "+v"(aw22[0]), "+v"(aw23[0])); \
    } while (0)
#define P2_SETUP2(OFF) do { apw = lb + (OFF) + (kq * 64 + 32 * b2 + i16) * 144 + qd * 8; \
      apr = lb + (OFF) + ((lane >> 4) * 64 + 8 * w + ((lane >> 3) & 1)) * 144 + c8 * 16; \
      for (int e = 0; e < 4; ++e) { const u32 v_ = ((lane & 7) == 2 * e ? 0x3f80u : 0u) | ((lane & 7) == 2 * e + 1 ? 0x3f800000u : 0u); \
        sel0[e] = (lane & 8) ? 0u : v_; sel1[e] = (lane & 8) ? v_ : 0u; } \
      asm volatile("" : "+v"(apw), "+v"(apr), "+v"(sel0), "+v"(sel1)); } while (0)
#define P2_WR(ACC, OFF, K) do { for (int pa = 0; pa < 2; ++pa) \
        *(LAS u32x2*)(apw + (OFF) + pa * 2304 + (K) * 32) = u32x2{PK(ACC[K][pa][0], ACC[K][pa][1]), PK(ACC[K][pa][2], ACC[K][pa][3])}; } while (0)
#define P2_READ(OFF) do { for (int k = 0; k < 4; ++k) pd[k] = LDS128(apr + (OFF) + k * 288); } while (0)
#define P2_RED(R) do { const f32x4 z_ = {0.f, 0.f, 0.f, 0.f}; \
      R[0] = MFMA16(sel1, pd[2], MFMA16(sel0, pd[0], z_)); R[1] = MFMA16(sel1, pd[3], MFMA16(sel0, pd[1], z_)); asm volatile("" : "+v"(R[0]), "+v"(R[1])); } while (0)
#define P2_S1(E) do { for (int e = (E); e < (E) + 4; ++e) { dsl[e] = __builtin_amdgcn_exp2f(rd[e >> 2][e & 3] * -1.4426950408889634f); asm volatile("" : "+v"(dsl[e])); } } while (0)
#define P2_S2(E) do { for (int e = (E); e < (E) + 4; ++e) { dsl[e] = __builtin_amdgcn_rcpf(1.0f + dsl[e]); asm volatile("" : "+v"(dsl[e])); } } while (0)
#define P2_S3() do { for (int e = 0; e < 8; ++e) { dsl[e] *= rd[e >> 2][e & 3]; asm volatile("" : "+v"(dsl[e])); } } while (0)
#define P2_XPREP() do { asm volatile("" : "+v"(xres)); \
      auto r0 = __builtin_amdgcn_permlane16_swap(xres[0], xres[2], false, false); auto r1 = __builtin_amdgcn_permlane16_swap(xres[1], xres[3], false, false); \
      xres = u32x4{r0[0], r1[0], r0[1], r1[1]}; asm volatile("" : "+v"(xres)); } while (0)
#define WO_READ(K) do { for (int c = 0; c < 2; ++c) { const u32 a_ = awo[(2 * ((K) & 1) + c) ^ ((K) & 2)] + 28 * ((K) >> 1) * 128; \
      wo_[K][c] = (K) < 6 ? TRF(a_, 2048) : TRF2(a_, a_ + (qd == 3 ? 1536 : 2048)); } } while (0)
#define P2_FINAL() do { u32 o_[4]; \
      for (int e = 0; e < 4; ++e) o_[e] = PK(BLO(xres[e]) + rs[e >> 1][2 * (e & 1)] * dsl[2 * e], BHI(xres[e]) + rs[e >> 1][2 * (e & 1) + 1] * dsl[2 * e + 1]); \
      auto r0 = __builtin_amdgcn_permlane16_swap(o_[0], o_[2], false, false); auto r1 = __builtin_amdgcn_permlane16_swap(o_[1], o_[3], false, false); \
      const u32x4 ov = {r0[0], r1[0], r0[1], r1[1]}; \
      __builtin_amdgcn_raw_buffer_store_b128(ov, rOut, (int)vxr, 0, 16); \
    } while (0)
  TOUCH_WQ(6);
  GLDS3(0, vx, rX, sx, 0);
  GLDS3(8192, vx, rX, sx, 57344);
  GLDS3(16384, vwq, rQKV, skv, 0);
  EPOCH_ISSUE();
  GLDS3(24576, vx, rX, sx, 128);
  GLDS3(32768, vx, rX, sx, 57472);
  GLDS3(40960, vwq, rQKV, skv, 172032);
  GLDS3(49152, vx, rX, sx, 256);
  GLDS3(57344, vx, rX, sx, 57600);
  GLDS3(65536, vwq, rQKV, skv, 344064);
  GLDS3(73728, vx, rX, sx, 384);
  TOUCH_W1(4);
  FFN_SETUP();
  EPOCH_LOAD();
  asm volatile("s_waitcnt vmcnt(7)\n\ts_barrier" :  :: "memory");
  xf[0] = LDS128(ax0[0] + 0);
  xf[1] = LDS128(ax0[0] + 2048);
  wk0 = TRF(aw0[0] + 16384, 512); SCHED();
  MZ16(a0[0][0], wk0, xf[0]); SCHED();
  trlo = TRH(aw1[0] + 16384); SCHED();
  MZ16(a0[0][1], wk0, xf[1]); SCHED();
  wk1 = TRJ(trlo, TRH(aw1[0] + 16384 + 512));
  GLDS3(81920, vx, rX, sx, 57728); SCHED();
  MZ16(a0[1][0], wk1, xf[0]); MZ16(a0[1][1], wk1, xf[1]); SCHED();
  xf[2] = LDS128(ax1[0] + 0);
  xf[3] = LDS128(ax1[0] + 2048);
  wk0 = TRF(aw0[0] + 20480, 512);
  GLDS3(90112, vwq, rQKV, skv, 516096); SCHED();
  MA16(a0[0][0], wk0, xf[2]); SCHED();
  trlo = TRH(aw1[0] + 20480); SCHED();
  MA16(a0[0][1], wk0, xf[3]); SCHED();
  wk1 = TRJ(trlo, TRH(aw1[0] + 20480 + 512)); SCHED();
  asm volatile("s_waitcnt lgkmcnt(0)\n\ts_waitcnt vmcnt(6)\n\ts_barrier" :  :: "memory");
  MA16(a0[1][0], wk1, xf[2]); MA16(a0[1][1], wk1, xf[3]); SCHED();
  xf[4] = LDS128(ax0[0] + 24576);
  xf[5] = LDS128(ax0[0] + 26624);
  wk0 = TRF(aw0[0] + 40960, 512);
  GLDS3(98304, vx, rX, sx, 512); SCHED();
  MA16(a0[0][0], wk0, xf[4]); SCHED();
  trlo = TRH(aw1[0] + 40960); SCHED();
  MA16(a0[0][1], wk0, xf[5]); SCHED();
  wk1 = TRJ(trlo, TRH(aw1[0] + 40960 + 512));
  GLDS3(106496, vx, rX, sx, 57856); SCHED();
  MA16(a0[1][0], wk1, xf[4]); MA16(a0[1][1], wk1, xf[5]); SCHED();
  xf[6] = LDS128(ax1[0] + 24576);
  xf[7] = LDS128(ax1[0] + 26624);
  wk0 = TRF(aw0[0] + 45056, 512);
  GLDS3(114688, vwq, rQKV, skv, 688128); SCHED();
  MA16(a0[0][0], wk0, xf[6]); SCHED();
  trlo = TRH(aw1[0] + 45056); SCHED();
  MA16(a0[0][1], wk0, xf[7]); SCHED();
  wk1 = TRJ(trlo, TRH(aw1[0] + 45056 + 512)); SCHED();
  asm volatile("s_waitcnt lgkmcnt(0)\n\ts_waitcnt vmcnt(6)\n\ts_barrier" :  :: "memory");
  MA16(a0[1][0], wk1, xf[6]); MA16(a0[1][1], wk1, xf[7]); SCHED();
  xf[8] = LDS128(ax0[0] + 49152);
  xf[9] = LDS128(ax0[0] + 51200);
  wk0 = TRF(aw0[1] + 0, 512);
  GLDS3(122880, vx, rX, sx, 640); SCHED();
  MA16(a0[0][0], wk0, xf[8]); SCHED();
  trlo = TRH(aw1[1] + 0); SCHED();
  MA16(a0[0][1], wk0, xf[9]); SCHED();
  wk1 = TRJ(trlo, TRH(aw1[1] + 0 + 512));
  GLDS3(131072, vx, rX, sx, 57984); SCHED();
  MA16(a0[1][0], wk1, xf[8]); MA16(a0[1][1], wk1, xf[9]); SCHED();
  xf[10] = LDS128(ax1[0] + 49152);
  xf[11] = LDS128(ax1[0] + 51200);
  wk0 = TRF(aw0[1] + 4096, 512);
  GLDS3(139264, vwq, rQKV, skv, 860160); SCHED();
  MA16(a0[0][0], wk0, xf[10]); SCHED();
  trlo = TRH(aw1[1] + 4096); SCHED();
  MA16(a0[0][1], wk0, xf[11]); SCHED();
  wk1 = TRJ(trlo, TRH(aw1[1] + 4096 + 512)); SCHED();
  asm volatile("s_waitcnt lgkmcnt(0)\n\ts_waitcnt vmcnt(6)\n\ts_barrier" :  :: "memory");
  MA16(a0[1][0], wk1, xf[10]); MA16(a0[1][1], wk1, xf[11]); SCHED();
  xf[12] = LDS128(ax0[1] + 8192);
  xf[13] = LDS128(ax0[1] + 10240);
  wk0 = TRF(aw0[1] + 24576, 512);
  GLDS3(147456, vx, rX, sx, 768); SCHED();
  MA16(a0[0][0], wk0, xf[12]); SCHED();
  trlo = TRH(aw1[1] + 24576); SCHED();
  MA16(a0[0][1], wk0, xf[13]); SCHED();
  wk1 = TRJ(trlo, TRH(aw1[1] + 24576 + 512));
  GLDS3(155648, vx, rX, sx, 58112); SCHED();
  MA16(a0[1][0], wk1, xf[12]); MA16(a0[1][1], wk1, xf[13]); SCHED();
  xf[14] = LDS128(ax1[1] + 8192);
  xf[15] = LDS128(ax1[1] + 10240);
  wk0 = TRF(aw0[1] + 28672, 512);
  GLDS3(0, vwq, rQKV, skv, 1032192); SCHED();
  MA16(a0[0][0], wk0, xf[14]); SCHED();
  trlo = TRH(aw1[1] + 28672); SCHED();
  MA16(a0[0][1], wk0, xf[15]); SCHED();
  wk1 = TRJ(trlo, TRH(aw1[1] + 28672 + 512)); SCHED();
  asm volatile("s_waitcnt lgkmcnt(0)\n\ts_waitcnt vmcnt(6)\n\ts_barrier" :  :: "memory");
  MA16(a0[1][0], wk1, xf[14]); MA16(a0[1][1], wk1, xf[15]); SCHED();
  xf[16] = LDS128(ax0[1] + 32768);
  xf[17] = LDS128(ax0[1] + 34816);
  wk0 = TRF(aw0[1] + 49152, 512);
  GLDS3(8192, vwg, rG, sgu, 0); SCHED();
  MA16(a0[0][0], wk0, xf[16]); SCHED();
  trlo = TRH(aw1[1] + 49152); SCHED();
  MA16(a0[0][1], wk0, xf[17]); SCHED();
  wk1 = TRJ(trlo, TRH(aw1[1] + 49152 + 512));
  GLDS3(16384, vwg, rU, sgu, 0); SCHED();
  MA16(a0[1][0], wk1, xf[16]); MA16(a0[1][1], wk1, xf[17]); SCHED();
  xf[18] = LDS128(ax1[1] + 32768);
  xf[19] = LDS128(ax1[1] + 34816);
  wk0 = TRF(aw0[1] + 53248, 512);
  GLDS3(24576, vwg, rG, sgu, 229376); SCHED();
  MA16(a0[0][0], wk0, xf[18]); SCHED();
  trlo = TRH(aw1[1] + 53248); SCHED();
  MA16(a0[0][1], wk0, xf[19]); SCHED();
  wk1 = TRJ(trlo, TRH(aw1[1] + 53248 + 512)); SCHED();
  asm volatile("s_waitcnt lgkmcnt(0)\n\ts_waitcnt vmcnt(6)\n\ts_barrier" :  :: "memory");
  MA16(a0[1][0], wk1, xf[18]); MA16(a0[1][1], wk1, xf[19]); SCHED();
  xf[20] = LDS128(ax0[1] + 57344);
  xf[21] = LDS128(ax0[1] + 59392);
  wk0 = TRF(aw0[2] + 8192, 512);
  GLDS3(32768, vwg, rU, sgu, 229376); SCHED();
  MA16(a0[0][0], wk0, xf[20]); SCHED();
  trlo = TRH(aw1[2] + 8192); SCHED();
  MA16(a0[0][1], wk0, xf[21]); SCHED();
  wk1 = TRJ(trlo, TRH(aw1[2] + 8192 + 512));
  GLDS3(40960, vwg, rG, sgu, 458752);
  FFN_IDENT(); SCHED();
  MA16(a0[1][0], wk1, xf[20]); MA16(a0[1][1], wk1, xf[21]); SCHED();
  xf[22] = LDS128(ax1[1] + 57344);
  xf[23] = LDS128(ax1[1] + 59392);
  wk0 = TRF(aw0[2] + 12288, 512);
  GLDS3(49152, vwg, rU, sgu, 458752); SCHED();
  MA16(a0[0][0], wk0, xf[22]); SCHED();
  trlo = TRH(aw1[2] + 12288); SCHED();
  MA16(a0[0][1], wk0, xf[23]); SCHED();
  wk1 = TRJ(trlo, TRH(aw1[2] + 12288 + 512)); SCHED();
  asm volatile("s_waitcnt lgkmcnt(0)\n\ts_waitcnt vmcnt(6)\n\ts_barrier" :  :: "memory");
  MA16(a0[1][0], wk1, xf[22]); MA16(a0[1][1], wk1, xf[23]); SCHED();
  xf[24] = LDS128(ax0[2] + 16384);
  xf[25] = LDS128(ax0[2] + 18432);
  wk0 = TRF(aw0[0] + 0, 512);
  GLDS3(57344, vwg, rG, sgu, 688128); SCHED();
  MA16(a0[0][0], wk0, xf[24]); SCHED();
  trlo = TRH(aw1[0] + 0); SCHED();
  MA16(a0[0][1], wk0, xf[25]); SCHED();
  wk1 = TRJ(trlo, TRH(aw1[0] + 0 + 512));
  GLDS3(65536, vwg, rU, sgu, 688128); SCHED();
  MA16(a0[1][0], wk1, xf[24]); MA16(a0[1][1], wk1, xf[25]); SCHED();
  xf[26] = LDS128(ax1[2] + 16384);
  xf[27] = LDS128(ax1[2] + 18432);
  wk0 = TRF(aw0[0] + 4096, 512);
  GLDS3(73728, vwg, rG, sgu, 917504); SCHED();
  MA16(a0[0][0], wk0, xf[26]); SCHED();
  trlo = TRH(aw1[0] + 4096); SCHED();
  MA16(a0[0][1], wk0, xf[27]); SCHED();
  wk1 = TRJ(trlo, TRH(aw1[0] + 4096 + 512)); SCHED();
  asm volatile("s_waitcnt lgkmcnt(0)\n\ts_waitcnt vmcnt(5)\n\ts_barrier" :  :: "memory");
  MA16(a0[1][0], wk1, xf[26]); SCHED();
  trlo = TRH(aw0[0] + 8192); SCHED();
  MA16(a0[1][1], wk1, xf[27]); SCHED();
  wf0[0] = TRJ(trlo, TRH(aw0[0] + 8192 + 512));
  wf0[1] = TRF(aw0[0] + 16384, 512);
  wf1[0] = TRF(aw1[0] + 8192, 512);
  wf1[1] = TRF(aw1[0] + 16384, 512);
  GLDS3(81920, vwg, rU, sgu, 917504); SCHED();
  TOUCH_W2(5);
  MZ16(aa[0][0][0], wf0[0], xf[0]); SCHED();
  trlo = TRH(aw0[0] + 12288); SCHED();
  MZ16(aa[0][0][1], wf0[0], xf[1]); SCHED();
  wf2[0] = TRJ(trlo, TRH(aw0[0] + 12288 + 512));
  GLDS3(90112, vwg, rG, sgu, 1146880); SCHED();
  MZ16(aa[1][0][0], wf0[1], xf[0]); SCHED();
  trlo = TRH(aw0[0] + 20480); SCHED();
  MZ16(aa[1][0][1], wf0[1], xf[1]); SCHED();
  wf2[1] = TRJ(trlo, TRH(aw0[0] + 20480 + 512)); SCHED();
  MZ16(aa[0][1][0], wf1[0], xf[0]); SCHED();
  trlo = TRH(aw1[0] + 12288); SCHED();
  MZ16(aa[0][1][1], wf1[0], xf[1]); SCHED();
  wf0[0] = TRJ(trlo, TRH(aw1[0] + 12288 + 512)); SCHED();
  MZ16(aa[1][1][0], wf1[1], xf[0]); SCHED();
  trlo = TRH(aw1[0] + 20480); SCHED();
  MZ16(aa[1][1][1], wf1[1], xf[1]); SCHED();
  wf0[1] = TRJ(trlo, TRH(aw1[0] + 20480 + 512)); SCHED();
  MA16(aa[0][0][0], wf2[0], xf[2]); SCHED();
  trlo = TRH(aw0[0] + 24576); SCHED();
  MA16(aa[0][0][1], wf2[0], xf[3]); SCHED();
  wf1[0] = TRJ(trlo, TRH(aw0[0] + 24576 + 512));
  GLDS3(98304, vwg, rU, sgu, 1146880); SCHED();
  MA16(aa[1][0][0], wf2[1], xf[2]); SCHED();
  trlo = TRH(aw0[0] + 32768); SCHED();
  MA16(aa[1][0][1], wf2[1], xf[3]); SCHED();
  wf1[1] = TRJ(trlo, TRH(aw0[0] + 32768 + 512));
  BLOAD4(gr, vgr0, rWS, 0, 0); SCHED();
  MA16(aa[0][1][0], wf0[0], xf[2]); SCHED();
  trlo = TRH(aw1[0] + 24576); SCHED();
  MA16(aa[0][1][1], wf0[0], xf[3]); SCHED();
  wf2[0] = TRJ(trlo, TRH(aw1[0] + 24576 + 512));
  GLDS3(106496, vwg, rG, sgu, 1376256); SCHED();
  MA16(aa[1][1][0], wf0[1], xf[2]); SCHED();
  trlo = TRH(aw1[0] + 32768); SCHED();
  MA16(aa[1][1][1], wf0[1], xf[3]); SCHED();
  wf2[1] = TRJ(trlo, TRH(aw1[0] + 32768 + 512)); SCHED();
  MA16(aa[0][0][0], wf1[0], xf[4]); SCHED();
  trlo = TRH(aw0[0] + 28672); SCHED();
  MA16(aa[0][0][1], wf1[0], xf[5]); SCHED();
  wf0[0] = TRJ(trlo, TRH(aw0[0] + 28672 + 512));
  GLDS3(114688, vwg, rU, sgu, 1376256); SCHED();
  MA16(aa[1][0][0], wf1[1], xf[4]); SCHED();
  trlo = TRH(aw0[0] + 36864); SCHED();
  MA16(aa[1][0][1], wf1[1], xf[5]); SCHED();
  wf0[1] = TRJ(trlo, TRH(aw0[0] + 36864 + 512)); SCHED();
  MA16(aa[0][1][0], wf2[0], xf[4]); SCHED();
  trlo = TRH(aw1[0] + 28672); SCHED();
  MA16(aa[0][1][1], wf2[0], xf[5]); SCHED();
  wf1[0] = TRJ(trlo, TRH(aw1[0] + 28672 + 512));
  wf1[1] = TRF(aw1[0] + 36864, 512); SCHED();
  MA16(aa[1][1][0], wf2[1], xf[4]); MA16(aa[1][1][1], wf2[1], xf[5]); SCHED();
  asm volatile("s_waitcnt lgkmcnt(0)\n\ts_waitcnt vmcnt(7)\n\ts_barrier" :  :: "memory");
  MA16(aa[0][0][0], wf0[0], xf[6]); SCHED();
  trlo = TRH(aw0[0] + 40960); SCHED();
  MA16(aa[0][0][1], wf0[0], xf[7]); SCHED();
  wf2[0] = TRJ(trlo, TRH(aw0[0] + 40960 + 512));
  GLDS3(0, vwg, rG, sgu, 128); SCHED();
  MA16(aa[1][0][0], wf0[1], xf[6]); SCHED();
  trlo = TRH(aw0[0] + 49152); SCHED();
  MA16(aa[1][0][1], wf0[1], xf[7]); SCHED();
  wf2[1] = TRJ(trlo, TRH(aw0[0] + 49152 + 512)); SCHED();
  MA16(aa[0][1][0], wf1[0], xf[6]); SCHED();
  trlo = TRH(aw1[0] + 40960); SCHED();
  MA16(aa[0][1][1], wf1[0], xf[7]); SCHED();
  wf0[0] = TRJ(trlo, TRH(aw1[0] + 40960 + 512));
  GLDS3(122880, vwg, rU, sgu, 128); SCHED();
  MA16(aa[1][1][0], wf1[1], xf[6]); SCHED();
  trlo = TRH(aw1[0] + 49152); SCHED();
  MA16(aa[1][1][1], wf1[1], xf[7]); SCHED();
  wf0[1] = TRJ(trlo, TRH(aw1[0] + 49152 + 512));
  GRAN_CHECK(vgr0, 0, 4);
  FFN_K0P(); SCHED();
  MA16(aa[0][0][0], wf2[0], xf[8]); SCHED();
  trlo = TRH(aw0[0] + 45056); SCHED();
  MA16(aa[0][0][1], wf2[0], xf[9]); SCHED();
  wf1[0] = TRJ(trlo, TRH(aw0[0] + 45056 + 512));
  GLDS3(131072, vwg, rG, sgu, 229504);
  FFN_K0(); SCHED();
  MA16(aa[1][0][0], wf2[1], xf[8]); SCHED();
  trlo = TRH(aw0[0] + 53248); SCHED();
  MA16(aa[1][0][1], wf2[1], xf[9]); SCHED();
  wf1[1] = TRJ(trlo, TRH(aw0[0] + 53248 + 512)); SCHED();
  MA16(aa[0][1][0], wf0[0], xf[8]); SCHED();
  trlo = TRH(aw1[0] + 45056); SCHED();
  MA16(aa[0][1][1], wf0[0], xf[9]); SCHED();
  wf2[0] = TRJ(trlo, TRH(aw1[0] + 45056 + 512));
  FFN_K1(); SCHED();
  MA16(aa[1][1][0], wf0[1], xf[8]); SCHED();
  trlo = TRH(aw1[0] + 53248); SCHED();
  MA16(aa[1][1][1], wf0[1], xf[9]); SCHED();
  wf2[1] = TRJ(trlo, TRH(aw1[0] + 53248 + 512)); SCHED();
  MA16(aa[0][0][0], wf1[0], xf[10]); SCHED();
  trlo = TRH(aw0[0] + 57344); SCHED();
  MA16(aa[0][0][1], wf1[0], xf[11]); SCHED();
  wf0[0] = TRJ(trlo, TRH(aw0[0] + 57344 + 512));
  GLDS3(139264, vwg, rU, sgu, 229504); SCHED();
  MA16(aa[1][0][0], wf1[1], xf[10]); SCHED();
  trlo = TRH(aw0[1] + 0); SCHED();
  MA16(aa[1][0][1], wf1[1], xf[11]); SCHED();
  wf0[1] = TRJ(trlo, TRH(aw0[1] + 0 + 512));
  FFN_ST0(); SCHED();
  MA16(aa[0][1][0], wf2[0], xf[10]); SCHED();
  trlo = TRH(aw1[0] + 57344); SCHED();
  MA16(aa[0][1][1], wf2[0], xf[11]); SCHED();
  wf1[0] = TRJ(trlo, TRH(aw1[0] + 57344 + 512));
  GLDS3(147456, vwg, rG, sgu, 458880); SCHED();
  MA16(aa[1][1][0], wf2[1], xf[10]); SCHED();
  trlo = TRH(aw1[1] + 0); SCHED();
  MA16(aa[1][1][1], wf2[1], xf[11]); SCHED();
  wf1[1] = TRJ(trlo, TRH(aw1[1] + 0 + 512)); SCHED();
  MA16(aa[0][0][0], wf0[0], xf[12]); SCHED();
  trlo = TRH(aw0[0] + 61440); SCHED();
  MA16(aa[0][0][1], wf0[0], xf[13]); SCHED();
  wf2[0] = TRJ(trlo, TRH(aw0[0] + 61440 + 512));
  GLDS3(155648, vwg, rU, sgu, 458880); SCHED();
  MA16(aa[1][0][0], wf0[1], xf[12]); SCHED();
  trlo = TRH(aw0[1] + 4096); SCHED();
  MA16(aa[1][0][1], wf0[1], xf[13]); SCHED();
  wf2[1] = TRJ(trlo, TRH(aw0[1] + 4096 + 512)); SCHED();
  MA16(aa[0][1][0], wf1[0], xf[12]); SCHED();
  trlo = TRH(aw1[0] + 61440); SCHED();
  MA16(aa[0][1][1], wf1[0], xf[13]); SCHED();
  wf0[0] = TRJ(trlo, TRH(aw1[0] + 61440 + 512));
  wf0[1] = TRF(aw1[1] + 4096, 512); SCHED();
  MA16(aa[1][1][0], wf1[1], xf[12]); MA16(aa[1][1][1], wf1[1], xf[13]); SCHED();
  asm volatile("s_waitcnt lgkmcnt(0)\n\ts_barrier" :  :: "memory");
  MA16(aa[0][0][0], wf2[0], xf[14]); SCHED();
  trlo = TRH(aw0[1] + 8192); SCHED();
  MA16(aa[0][0][1], wf2[0], xf[15]); SCHED();
  wf1[0] = TRJ(trlo, TRH(aw0[1] + 8192 + 512));
  GLDS3(8192, vw2, rWO, sw2, 0); SCHED();
  MA16(aa[1][0][0], wf2[1], xf[14]); SCHED();
  trlo = TRH(aw0[1] + 16384); SCHED();
  MA16(aa[1][0][1], wf2[1], xf[15]); SCHED();
  wf1[1] = TRJ(trlo, TRH(aw0[1] + 16384 + 512)); SCHED();
  MA16(aa[0][1][0], wf0[0], xf[14]); SCHED();
  trlo = TRH(aw1[1] + 8192); SCHED();
  MA16(aa[0][1][1], wf0[0], xf[15]); SCHED();
  wf2[0] = TRJ(trlo, TRH(aw1[1] + 8192 + 512));
  GLDS3(16384, vw2, rWO, sw2, 57344); SCHED();
  MA16(aa[1][1][0], wf0[1], xf[14]); SCHED();
  trlo = TRH(aw1[1] + 16384); SCHED();
  MA16(aa[1][1][1], wf0[1], xf[15]); SCHED();
  wf2[1] = TRJ(trlo, TRH(aw1[1] + 16384 + 512)); SCHED();
  MA16(aa[0][0][0], wf1[0], xf[16]); SCHED();
  trlo = TRH(aw0[1] + 12288); SCHED();
  MA16(aa[0][0][1], wf1[0], xf[17]); SCHED();
  wf0[0] = TRJ(trlo, TRH(aw0[1] + 12288 + 512));
  GLDS3(24576, vw2, rWO, sw2, 114688); SCHED();
  MA16(aa[1][0][0], wf1[1], xf[16]); SCHED();
  trlo = TRH(aw0[1] + 20480); SCHED();
  MA16(aa[1][0][1], wf1[1], xf[17]); SCHED();
  wf0[1] = TRJ(trlo, TRH(aw0[1] + 20480 + 512)); SCHED();
  MA16(aa[0][1][0], wf2[0], xf[16]); SCHED();
  trlo = TRH(aw1[1] + 12288); SCHED();
  MA16(aa[0][1][1], wf2[0], xf[17]); SCHED();
  wf1[0] = TRJ(trlo, TRH(aw1[1] + 12288 + 512)); SCHED();
  MA16(aa[1][1][0], wf2[1], xf[16]); SCHED();
  trlo = TRH(aw1[1] + 20480); SCHED();
  MA16(aa[1][1][1], wf2[1], xf[17]); SCHED();
  wf1[1] = TRJ(trlo, TRH(aw1[1] + 20480 + 512)); SCHED();
  MA16(aa[0][0][0], wf0[0], xf[18]); SCHED();
  trlo = TRH(aw0[1] + 24576); SCHED();
  MA16(aa[0][0][1], wf0[0], xf[19]); SCHED();
  wf2[0] = TRJ(trlo, TRH(aw0[1] + 24576 + 512));
  GLDS3(32768, vw2, rWO, sw2, 172032); SCHED();
  MA16(aa[1][0][0], wf0[1], xf[18]); SCHED();
  trlo = TRH(aw0[1] + 32768); SCHED();
  MA16(aa[1][0][1], wf0[1], xf[19]); SCHED();
  wf2[1] = TRJ(trlo, TRH(aw0[1] + 32768 + 512)); SCHED();
  MA16(aa[0][1][0], wf1[0], xf[18]); SCHED();
  trlo = TRH(aw1[1] + 24576); SCHED();
  MA16(aa[0][1][1], wf1[0], xf[19]); SCHED();
  wf0[0] = TRJ(trlo, TRH(aw1[1] + 24576 + 512));
  GLDS3(40960, vw2, rWO, sw2, 229376); SCHED();
  MA16(aa[1][1][0], wf1[1], xf[18]); SCHED();
  trlo = TRH(aw1[1] + 32768); SCHED();
  MA16(aa[1][1][1], wf1[1], xf[19]); SCHED();
  wf0[1] = TRJ(trlo, TRH(aw1[1] + 32768 + 512)); SCHED();
  MA16(aa[0][0][0], wf2[0], xf[20]); SCHED();
  trlo = TRH(aw0[1] + 28672); SCHED();
  MA16(aa[0][0][1], wf2[0], xf[21]); SCHED();
  wf1[0] = TRJ(trlo, TRH(aw0[1] + 28672 + 512));
  GLDS3(49152, vw2, rWO, sw2, 286720); SCHED();
  MA16(aa[1][0][0], wf2[1], xf[20]); SCHED();
  trlo = TRH(aw0[1] + 36864); SCHED();
  MA16(aa[1][0][1], wf2[1], xf[21]); SCHED();
  wf1[1] = TRJ(trlo, TRH(aw0[1] + 36864 + 512)); SCHED();
  MA16(aa[0][1][0], wf0[0], xf[20]); SCHED();
  trlo = TRH(aw1[1] + 28672); SCHED();
  MA16(aa[0][1][1], wf0[0], xf[21]); SCHED();
  wf2[0] = TRJ(trlo, TRH(aw1[1] + 28672 + 512));
  wf2[1] = TRF(aw1[1] + 36864, 512); SCHED();
  MA16(aa[1][1][0], wf0[1], xf[20]); MA16(aa[1][1][1], wf0[1], xf[21]); SCHED();
  BLOAD4(gr, vgr1, rWS, 0, 0); SCHED();
  asm volatile("s_waitcnt lgkmcnt(0)\n\ts_waitcnt vmcnt(9)\n\ts_barrier" :  :: "memory");
  FLAG_W(sfk + 0);
  MA16(aa[0][0][0], wf1[0], xf[22]); SCHED();
  trlo = TRH(aw0[1] + 40960); SCHED();
  MA16(aa[0][0][1], wf1[0], xf[23]); SCHED();
  wf0[0] = TRJ(trlo, TRH(aw0[1] + 40960 + 512));
  GLDS3(57344, vw2, rWO, sw2, 344064); SCHED();
  MA16(aa[1][0][0], wf1[1], xf[22]); SCHED();
  trlo = TRH(aw0[1] + 49152); SCHED();
  MA16(aa[1][0][1], wf1[1], xf[23]); SCHED();
  wf0[1] = TRJ(trlo, TRH(aw0[1] + 49152 + 512)); SCHED();
  MA16(aa[0][1][0], wf2[0], xf[22]); SCHED();
  trlo = TRH(aw1[1] + 40960); SCHED();
  MA16(aa[0][1][1], wf2[0], xf[23]); SCHED();
  wf1[0] = TRJ(trlo, TRH(aw1[1] + 40960 + 512));
  GLDS3(65536, vwg, rG, sgu, 688256); SCHED();
  MA16(aa[1][1][0], wf2[1], xf[22]); SCHED();
  trlo = TRH(aw1[1] + 49152); SCHED();
  MA16(aa[1][1][1], wf2[1], xf[23]); SCHED();
  wf1[1] = TRJ(trlo, TRH(aw1[1] + 49152 + 512)); SCHED();
  MA16(aa[0][0][0], wf0[0], xf[24]); SCHED();
  trlo = TRH(aw0[1] + 45056); SCHED();
  MA16(aa[0][0][1], wf0[0], xf[25]); SCHED();
  wf2[0] = TRJ(trlo, TRH(aw0[1] + 45056 + 512));
  GLDS3(73728, vwg, rU, sgu, 688256); SCHED();
  MA16(aa[1][0][0], wf0[1], xf[24]); SCHED();
  trlo = TRH(aw0[1] + 53248); SCHED();
  MA16(aa[1][0][1], wf0[1], xf[25]); SCHED();
  wf2[1] = TRJ(trlo, TRH(aw0[1] + 53248 + 512)); SCHED();
  MA16(aa[0][1][0], wf1[0], xf[24]); SCHED();
  trlo = TRH(aw1[1] + 45056); SCHED();
  MA16(aa[0][1][1], wf1[0], xf[25]); SCHED();
  wf0[0] = TRJ(trlo, TRH(aw1[1] + 45056 + 512));
  wf0[1] = TRF(aw1[1] + 53248, 512); SCHED();
  MA16(aa[1][1][0], wf1[1], xf[24]); MA16(aa[1][1][1], wf1[1], xf[25]); SCHED();
  asm volatile("s_waitcnt lgkmcnt(0)\n\ts_waitcnt vmcnt(2)\n\ts_barrier" :  :: "memory");
  MA16(aa[0][0][0], wf2[0], xf[26]); SCHED();
  trlo = TRH(aw0[0] + 0); SCHED();
  MA16(aa[0][0][1], wf2[0], xf[27]); SCHED();
  wf1[0] = TRJ(trlo, TRH(aw0[0] + 0 + 512));
  GLDS3(81920, vwg, rG, sgu, 917632); SCHED();
  MA16(aa[1][0][0], wf2[1], xf[26]); SCHED();
  trlo = TRH(aw0[1] + 57344); SCHED();
  MA16(aa[1][0][1], wf2[1], xf[27]); SCHED();
  wf1[1] = TRJ(trlo, TRH(aw0[1] + 57344 + 512)); SCHED();
  MA16(aa[0][1][0], wf0[0], xf[26]); SCHED();
  trlo = TRH(aw1[0] + 0); SCHED();
  MA16(aa[0][1][1], wf0[0], xf[27]); SCHED();
  wf2[0] = TRJ(trlo, TRH(aw1[0] + 0 + 512));
  GLDS3(90112, vwg, rU, sgu, 917632); SCHED();
  MA16(aa[1][1][0], wf0[1], xf[26]); SCHED();
  trlo = TRH(aw1[1] + 57344); SCHED();
  MA16(aa[1][1][1], wf0[1], xf[27]); SCHED();
  wf2[1] = TRJ(trlo, TRH(aw1[1] + 57344 + 512));
  GRAN_CHECK(vgr1, 0, 5);
  FFN_CP(); SCHED();
  asm volatile("" :: "v"(tw2), "v"(tw1), "v"(tw3));
  MZ16(ab[0][0][0], wf1[0], xf[0]); SCHED();
  trlo = TRH(aw0[0] + 4096); SCHED();
  MZ16(ab[0][0][1], wf1[0], xf[1]); SCHED();
  wf0[0] = TRJ(trlo, TRH(aw0[0] + 4096 + 512));
  GLDS3(98304, vwg, rG, sgu, 1147008);
  FFN_A0(); SCHED();
  MZ16(ab[1][0][0], wf1[1], xf[0]); SCHED();
  trlo = TRH(aw0[1] + 61440); SCHED();
  MZ16(ab[1][0][1], wf1[1], xf[1]); SCHED();
  wf0[1] = TRJ(trlo, TRH(aw0[1] + 61440 + 512)); SCHED();
  MZ16(ab[0][1][0], wf2[0], xf[0]); SCHED();
  trlo = TRH(aw1[0] + 4096); SCHED();
  MZ16(ab[0][1][1], wf2[0], xf[1]); SCHED();
  wf1[0] = TRJ(trlo, TRH(aw1[0] + 4096 + 512));
  FFN_A1(0); SCHED();
  MZ16(ab[1][1][0], wf2[1], xf[0]); SCHED();
  trlo = TRH(aw1[1] + 61440); SCHED();
  MZ16(ab[1][1][1], wf2[1], xf[1]); SCHED();
  wf1[1] = TRJ(trlo, TRH(aw1[1] + 61440 + 512));
  FFN_A1(1); SCHED();
  MA16(ab[0][0][0], wf0[0], xf[2]); SCHED();
  trlo = TRH(aw0[2] + 0); SCHED();
  MA16(ab[0][0][1], wf0[0], xf[3]); SCHED();
  wf2[0] = TRJ(trlo, TRH(aw0[2] + 0 + 512));
  GLDS3(106496, vwg, rU, sgu, 1147008);
  FFN_STA(); SCHED();
  MA16(ab[1][0][0], wf0[1], xf[2]); SCHED();
  trlo = TRH(aw0[2] + 8192); SCHED();
  MA16(ab[1][0][1], wf0[1], xf[3]); SCHED();
  wf2[1] = TRJ(trlo, TRH(aw0[2] + 8192 + 512));
  P2_SETUP(); SCHED();
  MA16(ab[0][1][0], wf1[0], xf[2]); SCHED();
  trlo = TRH(aw1[2] + 0); SCHED();
  MA16(ab[0][1][1], wf1[0], xf[3]); SCHED();
  wf0[0] = TRJ(trlo, TRH(aw1[2] + 0 + 512));
  GLDS3(114688, vwg, rG, sgu, 1376384); SCHED();
  MA16(ab[1][1][0], wf1[1], xf[2]); SCHED();
  trlo = TRH(aw1[2] + 8192); SCHED();
  MA16(ab[1][1][1], wf1[1], xf[3]); SCHED();
  wf0[1] = TRJ(trlo, TRH(aw1[2] + 8192 + 512));
  WO_READ(0);
  WO_READ(1); SCHED();
  MA16(ab[0][0][0], wf2[0], xf[4]); SCHED();
  trlo = TRH(aw0[2] + 4096); SCHED();
  MA16(ab[0][0][1], wf2[0], xf[5]); SCHED();
  wf1[0] = TRJ(trlo, TRH(aw0[2] + 4096 + 512));
  WO_READ(2); SCHED();
  MA16(ab[1][0][0], wf2[1], xf[4]); SCHED();
  trlo = TRH(aw0[2] + 12288); SCHED();
  MA16(ab[1][0][1], wf2[1], xf[5]); SCHED();
  wf1[1] = TRJ(trlo, TRH(aw0[2] + 12288 + 512));
  WO_READ(3); SCHED();
  MA16(ab[0][1][0], wf0[0], xf[4]); SCHED();
  trlo = TRH(aw1[2] + 4096); SCHED();
  MA16(ab[0][1][1], wf0[0], xf[5]); SCHED();
  wf2[0] = TRJ(trlo, TRH(aw1[2] + 4096 + 512));
  wf2[1] = TRF(aw1[2] + 12288, 512);
  WO_READ(4);
  WO_READ(5); SCHED();
  MA16(ab[1][1][0], wf0[1], xf[4]); MA16(ab[1][1][1], wf0[1], xf[5]); SCHED();
  WO_READ(6); SCHED();
  asm volatile("s_waitcnt lgkmcnt(0)\n\ts_barrier" :  :: "memory");
  MA16(ab[0][0][0], wf1[0], xf[6]); SCHED();
  trlo = TRH(aw0[2] + 16384); SCHED();
  MA16(ab[0][0][1], wf1[0], xf[7]); SCHED();
  wf0[0] = TRJ(trlo, TRH(aw0[2] + 16384 + 512));
  GLDS3(0, vwg, rU, sgu, 1376384);
  WO_READ(7); SCHED();
  MA16(ab[1][0][0], wf1[1], xf[6]); SCHED();
  trlo = TRH(aw0[2] + 24576); SCHED();
  MA16(ab[1][0][1], wf1[1], xf[7]); SCHED();
  wf0[1] = TRJ(trlo, TRH(aw0[2] + 24576 + 512)); SCHED();
  MA16(ab[0][1][0], wf2[0], xf[6]); SCHED();
  trlo = TRH(aw1[2] + 16384); SCHED();
  MA16(ab[0][1][1], wf2[0], xf[7]); SCHED();
  wf1[0] = TRJ(trlo, TRH(aw1[2] + 16384 + 512));
  GLDS3(8192, vw2, rWD, sw2, 0); SCHED();
  MA16(ab[1][1][0], wf2[1], xf[6]); SCHED();
  trlo = TRH(aw1[2] + 24576); SCHED();
  MA16(ab[1][1][1], wf2[1], xf[7]); SCHED();
  wf1[1] = TRJ(trlo, TRH(aw1[2] + 24576 + 512)); SCHED();
  MA16(ab[0][0][0], wf0[0], xf[8]); SCHED();
  trlo = TRH(aw0[2] + 20480); SCHED();
  MA16(ab[0][0][1], wf0[0], xf[9]); SCHED();
  wf2[0] = TRJ(trlo, TRH(aw0[2] + 20480 + 512));
  GLDS3(16384, vw2, rWD, sw2, 114688); SCHED();
  MA16(ab[1][0][0], wf0[1], xf[8]); SCHED();
  trlo = TRH(aw0[2] + 28672); SCHED();
  MA16(ab[1][0][1], wf0[1], xf[9]); SCHED();
  wf2[1] = TRJ(trlo, TRH(aw0[2] + 28672 + 512)); SCHED();
  MA16(ab[0][1][0], wf1[0], xf[8]); SCHED();
  trlo = TRH(aw1[2] + 20480); SCHED();
  MA16(ab[0][1][1], wf1[0], xf[9]); SCHED();
  wf0[0] = TRJ(trlo, TRH(aw1[2] + 20480 + 512));
  wf0[1] = TRF(aw1[2] + 28672, 512);
  GLDS3(24576, vw2, rWD, sw2, 229376); SCHED();
  MA16(ab[1][1][0], wf1[1], xf[8]); MA16(ab[1][1][1], wf1[1], xf[9]); SCHED();
  asm volatile("s_waitcnt lgkmcnt(0)\n\ts_waitcnt vmcnt(5)\n\ts_barrier" :  :: "memory");
  FLAG_W(sfk + 4);
  MA16(ab[0][0][0], wf2[0], xf[10]); SCHED();
  trlo = TRH(aw0[1] + 0); SCHED();
  MA16(ab[0][0][1], wf2[0], xf[11]); SCHED();
  wf1[0] = TRJ(trlo, TRH(aw0[1] + 0 + 512));
  GLDS3(32768, vw2, rWD, sw2, 344064); SCHED();
  MA16(ab[1][0][0], wf2[1], xf[10]); SCHED();
  trlo = TRH(aw0[1] + 8192); SCHED();
  MA16(ab[1][0][1], wf2[1], xf[11]); SCHED();
  wf1[1] = TRJ(trlo, TRH(aw0[1] + 8192 + 512)); SCHED();
  MA16(ab[0][1][0], wf0[0], xf[10]); SCHED();
  trlo = TRH(aw1[1] + 0); SCHED();
  MA16(ab[0][1][1], wf0[0], xf[11]); SCHED();
  wf2[0] = TRJ(trlo, TRH(aw1[1] + 0 + 512));
  GLDS3(40960, vw2, rWD, sw2, 458752); SCHED();
  MA16(ab[1][1][0], wf0[1], xf[10]); SCHED();
  trlo = TRH(aw1[1] + 8192); SCHED();
  MA16(ab[1][1][1], wf0[1], xf[11]); SCHED();
  wf2[1] = TRJ(trlo, TRH(aw1[1] + 8192 + 512)); SCHED();
  MA16(ab[0][0][0], wf1[0], xf[12]); SCHED();
  trlo = TRH(aw0[1] + 4096); SCHED();
  MA16(ab[0][0][1], wf1[0], xf[13]); SCHED();
  wf0[0] = TRJ(trlo, TRH(aw0[1] + 4096 + 512));
  GLDS3(49152, vw2, rWD, sw2, 573440); SCHED();
  MA16(ab[1][0][0], wf1[1], xf[12]); SCHED();
  trlo = TRH(aw0[1] + 12288); SCHED();
  MA16(ab[1][0][1], wf1[1], xf[13]); SCHED();
  wf0[1] = TRJ(trlo, TRH(aw0[1] + 12288 + 512)); SCHED();
  MA16(ab[0][1][0], wf2[0], xf[12]); SCHED();
  trlo = TRH(aw1[1] + 4096); SCHED();
  MA16(ab[0][1][1], wf2[0], xf[13]); SCHED();
  wf1[0] = TRJ(trlo, TRH(aw1[1] + 4096 + 512));
  GLDS3(122880, vw2, rWD, sw2, 688128); SCHED();
  MA16(ab[1][1][0], wf2[1], xf[12]); SCHED();
  trlo = TRH(aw1[1] + 12288); SCHED();
  MA16(ab[1][1][1], wf2[1], xf[13]); SCHED();
  wf1[1] = TRJ(trlo, TRH(aw1[1] + 12288 + 512)); SCHED();
  MA16(ab[0][0][0], wf0[0], xf[14]); SCHED();
  trlo = TRH(aw0[1] + 16384); SCHED();
  MA16(ab[0][0][1], wf0[0], xf[15]); SCHED();
  wf2[0] = TRJ(trlo, TRH(aw0[1] + 16384 + 512));
  GLDS3(131072, vw2, rWD, sw2, 802816); SCHED();
  MA16(ab[1][0][0], wf0[1], xf[14]); SCHED();
  trlo = TRH(aw0[1] + 24576); SCHED();
  MA16(ab[1][0][1], wf0[1], xf[15]); SCHED();
  wf2[1] = TRJ(trlo, TRH(aw0[1] + 24576 + 512)); SCHED();
  MA16(ab[0][1][0], wf1[0], xf[14]); SCHED();
  trlo = TRH(aw1[1] + 16384); SCHED();
  MA16(ab[0][1][1], wf1[0], xf[15]); SCHED();
  wf0[0] = TRJ(trlo, TRH(aw1[1] + 16384 + 512));
  GLDS3(139264, vw2, rWD, sw2, 917504); SCHED();
  MA16(ab[1][1][0], wf1[1], xf[14]); SCHED();
  trlo = TRH(aw1[1] + 24576); SCHED();
  MA16(ab[1][1][1], wf1[1], xf[15]); SCHED();
  wf0[1] = TRJ(trlo, TRH(aw1[1] + 24576 + 512)); SCHED();
  MA16(ab[0][0][0], wf2[0], xf[16]); SCHED();
  trlo = TRH(aw0[1] + 20480); SCHED();
  MA16(ab[0][0][1], wf2[0], xf[17]); SCHED();
  wf1[0] = TRJ(trlo, TRH(aw0[1] + 20480 + 512));
  GLDS3(147456, vw2, rWD, sw2, 1032192); SCHED();
  MA16(ab[1][0][0], wf2[1], xf[16]); SCHED();
  trlo = TRH(aw0[1] + 28672); SCHED();
  MA16(ab[1][0][1], wf2[1], xf[17]); SCHED();
  wf1[1] = TRJ(trlo, TRH(aw0[1] + 28672 + 512)); SCHED();
  MA16(ab[0][1][0], wf0[0], xf[16]); SCHED();
  trlo = TRH(aw1[1] + 20480); SCHED();
  MA16(ab[0][1][1], wf0[0], xf[17]); SCHED();
  wf2[0] = TRJ(trlo, TRH(aw1[1] + 20480 + 512));
  wf2[1] = TRF(aw1[1] + 28672, 512);
  GLDS3(57344, vw2, rWD, sw2, 1146880); SCHED();
  MA16(ab[1][1][0], wf0[1], xf[16]); MA16(ab[1][1][1], wf0[1], xf[17]); SCHED();
  BLOAD4P(xres, vxr, rX, 0, 0); SCHED();
  asm volatile("s_waitcnt lgkmcnt(0)\n\ts_barrier" :  :: "memory");
  MA16(ab[0][0][0], wf1[0], xf[18]); SCHED();
  trlo = TRH(aw0[1] + 32768); SCHED();
  MA16(ab[0][0][1], wf1[0], xf[19]); SCHED();
  wf0[0] = TRJ(trlo, TRH(aw0[1] + 32768 + 512));
  GLDS3(65536, vw2, rWD, sw2, 1261568); SCHED();
  MA16(ab[1][0][0], wf1[1], xf[18]); SCHED();
  trlo = TRH(aw0[1] + 40960); SCHED();
  MA16(ab[1][0][1], wf1[1], xf[19]); SCHED();
  wf0[1] = TRJ(trlo, TRH(aw0[1] + 40960 + 512)); SCHED();
  MA16(ab[0][1][0], wf2[0], xf[18]); SCHED();
  trlo = TRH(aw1[1] + 32768); SCHED();
  MA16(ab[0][1][1], wf2[0], xf[19]); SCHED();
  wf1[0] = TRJ(trlo, TRH(aw1[1] + 32768 + 512));
  GLDS3(73728, vw2, rWD, sw2, 1376256); SCHED();
  MA16(ab[1][1][0], wf2[1], xf[18]); SCHED();
  trlo = TRH(aw1[1] + 40960); SCHED();
  MA16(ab[1][1][1], wf2[1], xf[19]); SCHED();
  wf1[1] = TRJ(trlo, TRH(aw1[1] + 40960 + 512));
  BLOAD4(gr, vgr1, rWS, 0, 1024); SCHED();
  MA16(ab[0][0][0], wf0[0], xf[20]); SCHED();
  trlo = TRH(aw0[1] + 36864); SCHED();
  MA16(ab[0][0][1], wf0[0], xf[21]); SCHED();
  wf2[0] = TRJ(trlo, TRH(aw0[1] + 36864 + 512));
  GLDS3(81920, vw2, rWD, sw2, 1490944); SCHED();
  MA16(ab[1][0][0], wf0[1], xf[20]); SCHED();
  trlo = TRH(aw0[1] + 45056); SCHED();
  MA16(ab[1][0][1], wf0[1], xf[21]); SCHED();
  wf2[1] = TRJ(trlo, TRH(aw0[1] + 45056 + 512)); SCHED();
  MA16(ab[0][1][0], wf1[0], xf[20]); SCHED();
  trlo = TRH(aw1[1] + 36864); SCHED();
  MA16(ab[0][1][1], wf1[0], xf[21]); SCHED();
  wf0[0] = TRJ(trlo, TRH(aw1[1] + 36864 + 512));
  wf0[1] = TRF(aw1[1] + 45056, 512);
  GLDS3(90112, vw2, rWD, sw2, 57344); SCHED();
  MA16(ab[1][1][0], wf1[1], xf[20]); MA16(ab[1][1][1], wf1[1], xf[21]); SCHED();
  POLL_W(pl, vp1); SCHED();
  asm volatile("s_waitcnt lgkmcnt(0)\n\ts_waitcnt vmcnt(17)\n\ts_barrier" :  :: "memory");
  MA16(ab[0][0][0], wf2[0], xf[22]); SCHED();
  trlo = TRH(aw0[1] + 49152); SCHED();
  MA16(ab[0][0][1], wf2[0], xf[23]); SCHED();
  wf1[0] = TRJ(trlo, TRH(aw0[1] + 49152 + 512));
  GLDS3(98304, vw2, rWD, sw2, 172032); SCHED();
  MA16(ab[1][0][0], wf2[1], xf[22]); SCHED();
  trlo = TRH(aw0[0] + 0); SCHED();
  MA16(ab[1][0][1], wf2[1], xf[23]); SCHED();
  wf1[1] = TRJ(trlo, TRH(aw0[0] + 0 + 512)); SCHED();
  MA16(ab[0][1][0], wf0[0], xf[22]); SCHED();
  trlo = TRH(aw1[1] + 49152); SCHED();
  MA16(ab[0][1][1], wf0[0], xf[23]); SCHED();
  wf2[0] = TRJ(trlo, TRH(aw1[1] + 49152 + 512));
  GLDS3(106496, vw2, rWD, sw2, 286720); SCHED();
  MA16(ab[1][1][0], wf0[1], xf[22]); SCHED();
  trlo = TRH(aw1[0] + 0); SCHED();
  MA16(ab[1][1][1], wf0[1], xf[23]); SCHED();
  wf2[1] = TRJ(trlo, TRH(aw1[0] + 0 + 512)); SCHED();
  MA16(ab[0][0][0], wf1[0], xf[24]); SCHED();
  trlo = TRH(aw0[1] + 53248); SCHED();
  MA16(ab[0][0][1], wf1[0], xf[25]); SCHED();
  wf0[0] = TRJ(trlo, TRH(aw0[1] + 53248 + 512));
  GLDS3(155648, vw2, rWD, sw2, 401408); SCHED();
  MA16(ab[1][0][0], wf1[1], xf[24]); SCHED();
  trlo = TRH(aw0[0] + 4096); SCHED();
  MA16(ab[1][0][1], wf1[1], xf[25]); SCHED();
  wf0[1] = TRJ(trlo, TRH(aw0[0] + 4096 + 512)); SCHED();
  MA16(ab[0][1][0], wf2[0], xf[24]); SCHED();
  trlo = TRH(aw1[1] + 53248); SCHED();
  MA16(ab[0][1][1], wf2[0], xf[25]); SCHED();
  wf1[0] = TRJ(trlo, TRH(aw1[1] + 53248 + 512));
  wf1[1] = TRF(aw1[0] + 4096, 512); SCHED();
  MA16(ab[1][1][0], wf2[1], xf[24]); MA16(ab[1][1][1], wf2[1], xf[25]); SCHED();
  CHECK_W(pl, vp1, 3);
  asm volatile("s_waitcnt lgkmcnt(0)\n\ts_waitcnt vmcnt(18)\n\ts_barrier" :  :: "memory");
  MA16(ab[0][0][0], wf0[0], xf[26]); MA16(ab[0][0][1], wf0[0], xf[27]); SCHED();
  GLDS3(0, vw2, rWD, sw2, 516096); SCHED();
  MA16(ab[1][0][0], wf0[1], xf[26]); MA16(ab[1][0][1], wf0[1], xf[27]); SCHED();
  GLDS3(114688, vw2, rWD, sw2, 630784);
  GRAN_CHECK(vgr1, 1024, 7);
  FFN_CP(); SCHED();
  MA16(ab[0][1][0], wf1[0], xf[26]); MA16(ab[0][1][1], wf1[0], xf[27]); SCHED(); SCHED();
  MA16(ab[1][1][0], wf1[1], xf[26]); MA16(ab[1][1][1], wf1[1], xf[27]); SCHED(); SCHED();
  FFN_EPIB();
  wf0[0] = TRF(aw20[0] + 8192, 2048);
  wf0[1] = TRF(aw21[0] + 8192, 2048); SCHED();
  BLOAD4(hf[0], vh, rWS, sh + 0, 0);
  BLOAD4(hf[1], vh, rWS, sh + 0, 1024);
  BLOAD4(hf[2], vh, rWS, sh + 8192, 0);
  BLOAD4(hf[3], vh, rWS, sh + 8192, 1024);
  BLOAD4(hf[4], vh, rWS, sh + 16384, 0);
  BLOAD4(hf[5], vh, rWS, sh + 16384, 1024);
  P2_SETUP2(57344); P2_XPREP(); SCHED();
  asm volatile("s_waitcnt vmcnt(4)" : "+v"(hf[0]), "+v"(hf[1]) :: "memory");
  MZ16(ad[0][0], wf0[0], hf[0]); SCHED();
  trlo = TRH(aw22[0] + 8192); SCHED();
  MZ16(ad[0][1], wf0[0], hf[1]); SCHED();
  wf1[0] = TRJ(trlo, TRH(aw22[0] + 8192 + 2048));
  wf1[1] = TRF(aw23[0] + 8192, 2048);
  BLOAD4(hf[6], vh, rWS, sh + 24576, 0); SCHED();
  MZ16(ad[1][0], wf0[1], hf[0]); MZ16(ad[1][1], wf0[1], hf[1]); SCHED();
  BLOAD4(hf[7], vh, rWS, sh + 24576, 1024); SCHED();
  asm volatile("s_waitcnt lgkmcnt(0)\n\ts_barrier" :  :: "memory");
  FLAG_W(sfk + 8);
  MZ16(ad[2][0], wf1[0], hf[0]); SCHED();
  trlo = TRH(aw20[0] + 24576); SCHED();
  MZ16(ad[2][1], wf1[0], hf[1]); SCHED();
  wf0[0] = TRJ(trlo, TRH(aw20[0] + 24576 + 2048));
  wf0[1] = TRF(aw21[0] + 24576, 2048);
  BLOAD4(hf[8], vh, rWS, sh + 32768, 0); SCHED();
  MZ16(ad[3][0], wf1[1], hf[0]); MZ16(ad[3][1], wf1[1], hf[1]); SCHED();
  BLOAD4(hf[9], vh, rWS, sh + 32768, 1024); SCHED();
  asm volatile("s_waitcnt vmcnt(6)" : "+v"(hf[2]), "+v"(hf[3]) :: "memory");
  MA16(ad[0][0], wf0[0], hf[2]); SCHED();
  trlo = TRH(aw22[0] + 24576); SCHED();
  MA16(ad[0][1], wf0[0], hf[3]); SCHED();
  wf1[0] = TRJ(trlo, TRH(aw22[0] + 24576 + 2048));
  wf1[1] = TRF(aw23[0] + 24576, 2048);
  BLOAD4(hf[10], vh, rWS, sh + 40960, 0); SCHED();
  MA16(ad[1][0], wf0[1], hf[2]); MA16(ad[1][1], wf0[1], hf[3]); SCHED();
  BLOAD4(hf[11], vh, rWS, sh + 40960, 1024); SCHED();
  MA16(ad[2][0], wf1[0], hf[2]); SCHED();
  trlo = TRH(aw20[0] + 40960); SCHED();
  MA16(ad[2][1], wf1[0], hf[3]); SCHED();
  wf0[0] = TRJ(trlo, TRH(aw20[0] + 40960 + 2048));
  wf0[1] = TRF(aw21[0] + 40960, 2048);
  BLOAD4(hf[12], vh, rWS, sh + 49152, 0); SCHED();
  MA16(ad[3][0], wf1[1], hf[2]); MA16(ad[3][1], wf1[1], hf[3]); SCHED();
  BLOAD4(hf[13], vh, rWS, sh + 49152, 1024); SCHED();
  asm volatile("s_waitcnt vmcnt(8)" : "+v"(hf[4]), "+v"(hf[5]) :: "memory");
  MA16(ad[0][0], wf0[0], hf[4]); SCHED();
  trlo = TRH(aw22[0] + 40960); SCHED();
  MA16(ad[0][1], wf0[0], hf[5]); SCHED();
  wf1[0] = TRJ(trlo, TRH(aw22[0] + 40960 + 2048));
  wf1[1] = TRF(aw23[0] + 40960, 2048);
  GLDS3(8192, vw2, rWD, sw2, 745472); SCHED();
  MA16(ad[1][0], wf0[1], hf[4]); MA16(ad[1][1], wf0[1], hf[5]); SCHED();
  GLDS3(16384, vw2, rWD, sw2, 860160); SCHED();
  asm volatile("s_waitcnt lgkmcnt(0)\n\ts_barrier" :  :: "memory");
  MA16(ad[2][0], wf1[0], hf[4]); SCHED();
  trlo = TRH(aw20[1] + 57344); SCHED();
  MA16(ad[2][1], wf1[0], hf[5]); SCHED();
  wf0[0] = TRJ(trlo, TRH(aw20[1] + 57344 + 2048));
  wf0[1] = TRF(aw21[1] + 57344, 2048);
  GLDS3(24576, vw2, rWD, sw2, 974848); SCHED();
  MA16(ad[3][0], wf1[1], hf[4]); MA16(ad[3][1], wf1[1], hf[5]); SCHED();
  GLDS3(32768, vw2, rWD, sw2, 1089536); SCHED();
  asm volatile("s_waitcnt vmcnt(10)" : "+v"(hf[6]), "+v"(hf[7]) :: "memory");
  MA16(ad[0][0], wf0[0], hf[6]); SCHED();
  trlo = TRH(aw22[1] + 57344); SCHED();
  MA16(ad[0][1], wf0[0], hf[7]); SCHED();
  wf1[0] = TRJ(trlo, TRH(aw22[1] + 57344 + 2048));
  wf1[1] = TRF(aw23[1] + 57344, 2048);
  vp1 += 4;
  POLL_W(pl, vp1);
  GLDS3(40960, vw2, rWD, sw2, 1204224); SCHED();
  MA16(ad[1][0], wf0[1], hf[6]); MA16(ad[1][1], wf0[1], hf[7]); SCHED();
  GLDS3(49152, vw2, rWD, sw2, 1318912); SCHED();
  MA16(ad[2][0], wf1[0], hf[6]); SCHED();
  trlo = TRH(aw20[2] + 8192); SCHED();
  MA16(ad[2][1], wf1[0], hf[7]); SCHED();
  wf0[0] = TRJ(trlo, TRH(aw20[2] + 8192 + 2048));
  wf0[1] = TRF(aw21[2] + 8192, 2048); SCHED();
  MA16(ad[3][0], wf1[1], hf[6]); MA16(ad[3][1], wf1[1], hf[7]); SCHED(); SCHED();
  asm volatile("s_waitcnt vmcnt(10)" : "+v"(hf[8]), "+v"(hf[9]) :: "memory");
  MA16(ad[0][0], wf0[0], hf[8]); SCHED();
  trlo = TRH(aw22[2] + 8192); SCHED();
  MA16(ad[0][1], wf0[0], hf[9]); SCHED();
  wf1[0] = TRJ(trlo, TRH(aw22[2] + 8192 + 2048));
  wf1[1] = TRF(aw23[2] + 8192, 2048); SCHED();
  MA16(ad[1][0], wf0[1], hf[8]); MA16(ad[1][1], wf0[1], hf[9]); SCHED(); SCHED();
  CHECK_W(pl, vp1, 2);
  asm volatile("s_waitcnt lgkmcnt(0)\n\ts_barrier" :  :: "memory");
  MA16(ad[2][0], wf1[0], hf[8]); SCHED();
  trlo = TRH(aw20[0] + 57344); SCHED();
  MA16(ad[2][1], wf1[0], hf[9]); SCHED();
  wf0[0] = TRJ(trlo, TRH(aw20[0] + 57344 + 2048));
  wf0[1] = TRF(aw21[0] + 57344, 2048);
  BLOAD4(hf[14], vh, rWS, sh + 3670016, 0); SCHED();
  MA16(ad[3][0], wf1[1], hf[8]); MA16(ad[3][1], wf1[1], hf[9]); SCHED();
  GLDS3(122880, vw2, rWD, sw2, 1433600); SCHED();
  asm volatile("s_waitcnt vmcnt(10)" : "+v"(hf[10]), "+v"(hf[11]) :: "memory");
  MA16(ad[0][0], wf0[0], hf[10]); SCHED();
  trlo = TRH(aw22[0] + 57344); SCHED();
  MA16(ad[0][1], wf0[0], hf[11]); SCHED();
  wf1[0] = TRJ(trlo, TRH(aw22[0] + 57344 + 2048));
  wf1[1] = TRF(aw23[0] + 57344, 2048);
  BLOAD4(hf[15], vh, rWS, sh + 3670016, 1024); SCHED();
  MA16(ad[1][0], wf0[1], hf[10]); MA16(ad[1][1], wf0[1], hf[11]); SCHED();
  GLDS3(131072, vw2, rWD, sw2, 1548288); SCHED();
  MA16(ad[2][0], wf1[0], hf[10]); SCHED();
  trlo = TRH(aw20[1] + 8192); SCHED();
  MA16(ad[2][1], wf1[0], hf[11]); SCHED();
  wf0[0] = TRJ(trlo, TRH(aw20[1] + 8192 + 2048));
  wf0[1] = TRF(aw21[1] + 8192, 2048);
  BLOAD4(hf[16], vh, rWS, sh + 3678208, 0); SCHED();
  MA16(ad[3][0], wf1[1], hf[10]); MA16(ad[3][1], wf1[1], hf[11]); SCHED(); SCHED();
  asm volatile("s_waitcnt vmcnt(11)" : "+v"(hf[12]), "+v"(hf[13]) :: "memory");
  MA16(ad[0][0], wf0[0], hf[12]); SCHED();
  trlo = TRH(aw22[1] + 8192); SCHED();
  MA16(ad[0][1], wf0[0], hf[13]); SCHED();
  wf1[0] = TRJ(trlo, TRH(aw22[1] + 8192 + 2048));
  wf1[1] = TRF(aw23[1] + 8192, 2048);
  BLOAD4(hf[17], vh, rWS, sh + 3678208, 1024); SCHED();
  MA16(ad[1][0], wf0[1], hf[12]); MA16(ad[1][1], wf0[1], hf[13]); SCHED(); SCHED();
  MA16(ad[2][0], wf1[0], hf[12]); SCHED();
  trlo = TRH(aw20[1] + 24576); SCHED();
  MA16(ad[2][1], wf1[0], hf[13]); SCHED();
  wf0[0] = TRJ(trlo, TRH(aw20[1] + 24576 + 2048));
  wf0[1] = TRF(aw21[1] + 24576, 2048);
  BLOAD4(hf[18], vh, rWS, sh + 3686400, 0); SCHED();
  MA16(ad[3][0], wf1[1], hf[12]); MA16(ad[3][1], wf1[1], hf[13]); SCHED(); SCHED();
  asm volatile("s_waitcnt vmcnt(4)" : "+v"(hf[14]), "+v"(hf[15]) :: "memory");
  MA16(ad[0][0], wf0[0], hf[14]); SCHED();
  trlo = TRH(aw22[1] + 24576); SCHED();
  MA16(ad[0][1], wf0[0], hf[15]); SCHED();
  wf1[0] = TRJ(trlo, TRH(aw22[1] + 24576 + 2048));
  wf1[1] = TRF(aw23[1] + 24576, 2048);
  BLOAD4(hf[19], vh, rWS, sh + 3686400, 1024); SCHED();
  MA16(ad[1][0], wf0[1], hf[14]); MA16(ad[1][1], wf0[1], hf[15]); SCHED();
  BLOAD4(hf[20], vh, rWS, sh + 3694592, 0); SCHED();
  MA16(ad[2][0], wf1[0], hf[14]); SCHED();
  trlo = TRH(aw20[0] + ((kq >> 1) ? 147456 : 106496)); SCHED();
  MA16(ad[2][1], wf1[0], hf[15]); SCHED();
  wf0[0] = TRJ(trlo, TRH(aw20[0] + ((kq >> 1) ? 147456 : 106496) + 2048));
  wf0[1] = TRF(aw21[0] + ((kq >> 1) ? 147456 : 106496), 2048);
  BLOAD4(hf[21], vh, rWS, sh + 3694592, 1024); SCHED();
  MA16(ad[3][0], wf1[1], hf[14]); MA16(ad[3][1], wf1[1], hf[15]); SCHED(); SCHED();
  asm volatile("s_waitcnt vmcnt(4)" : "+v"(hf[16]), "+v"(hf[17]) :: "memory");
  MA16(ad[0][0], wf0[0], hf[16]); SCHED();
  trlo = TRH(aw22[0] + ((kq >> 1) ? 147456 : 106496)); SCHED();
  MA16(ad[0][1], wf0[0], hf[17]); SCHED();
  wf1[0] = TRJ(trlo, TRH(aw22[0] + ((kq >> 1) ? 147456 : 106496) + 2048));
  wf1[1] = TRF(aw23[0] + ((kq >> 1) ? 147456 : 106496), 2048);
  BLOAD4(hf[22], vh, rWS, sh + 3702784, 0); SCHED();
  MA16(ad[1][0], wf0[1], hf[16]); MA16(ad[1][1], wf0[1], hf[17]); SCHED();
  BLOAD4(hf[23], vh, rWS, sh + 3702784, 1024); SCHED();
  asm volatile("s_waitcnt lgkmcnt(0)\n\ts_barrier" :  :: "memory");
  MA16(ad[2][0], wf1[0], hf[16]); SCHED();
  trlo = TRH(aw20[0] + ((kq >> 1) ? 106496 : 0)); SCHED();
  MA16(ad[2][1], wf1[0], hf[17]); SCHED();
  wf0[0] = TRJ(trlo, TRH(aw20[0] + ((kq >> 1) ? 106496 : 0) + 2048));
  wf0[1] = TRF(aw21[0] + ((kq >> 1) ? 106496 : 0), 2048);
  BLOAD1(pl3, vp3);
  BLOAD4(hf[24], vh, rWS, sh + 3710976, 0); SCHED();
  MA16(ad[3][0], wf1[1], hf[16]); MA16(ad[3][1], wf1[1], hf[17]); SCHED(); SCHED();
  asm volatile("s_waitcnt vmcnt(6)" : "+v"(hf[18]), "+v"(hf[19]) :: "memory");
  MA16(ad[0][0], wf0[0], hf[18]); SCHED();
  trlo = TRH(aw22[0] + ((kq >> 1) ? 106496 : 0)); SCHED();
  MA16(ad[0][1], wf0[0], hf[19]); SCHED();
  wf1[0] = TRJ(trlo, TRH(aw22[0] + ((kq >> 1) ? 106496 : 0) + 2048));
  wf1[1] = TRF(aw23[0] + ((kq >> 1) ? 106496 : 0), 2048);
  BLOAD4(hf[25], vh, rWS, sh + 3710976, 1024); SCHED();
  MA16(ad[1][0], wf0[1], hf[18]); MA16(ad[1][1], wf0[1], hf[19]); SCHED(); SCHED();
  MA16(ad[2][0], wf1[0], hf[18]); SCHED();
  trlo = TRH(aw20[0] + 8192); SCHED();
  MA16(ad[2][1], wf1[0], hf[19]); SCHED();
  wf0[0] = TRJ(trlo, TRH(aw20[0] + 8192 + 2048));
  wf0[1] = TRF(aw21[0] + 8192, 2048);
  BLOAD4(hf[26], vh, rWS, sh + 3719168, 0); SCHED();
  MA16(ad[3][0], wf1[1], hf[18]); MA16(ad[3][1], wf1[1], hf[19]); SCHED(); SCHED();
  asm volatile("s_waitcnt vmcnt(6)" : "+v"(hf[20]), "+v"(hf[21]) :: "memory");
  MA16(ad[0][0], wf0[0], hf[20]); SCHED();
  trlo = TRH(aw22[0] + 8192); SCHED();
  MA16(ad[0][1], wf0[0], hf[21]); SCHED();
  wf1[0] = TRJ(trlo, TRH(aw22[0] + 8192 + 2048));
  wf1[1] = TRF(aw23[0] + 8192, 2048);
  BLOAD4(hf[27], vh, rWS, sh + 3719168, 1024); SCHED();
  MA16(ad[1][0], wf0[1], hf[20]); MA16(ad[1][1], wf0[1], hf[21]); SCHED(); SCHED();
  MA16(ad[2][0], wf1[0], hf[20]); SCHED();
  trlo = TRH(aw20[0] + 24576); SCHED();
  MA16(ad[2][1], wf1[0], hf[21]); SCHED();
  wf0[0] = TRJ(trlo, TRH(aw20[0] + 24576 + 2048));
  wf0[1] = TRF(aw21[0] + 24576, 2048); SCHED();
  MA16(ad[3][0], wf1[1], hf[20]); MA16(ad[3][1], wf1[1], hf[21]); SCHED(); SCHED();
  asm volatile("s_waitcnt vmcnt(5)" : "+v"(hf[22]), "+v"(hf[23]) :: "memory");
  MA16(ad[0][0], wf0[0], hf[22]); SCHED();
  trlo = TRH(aw22[0] + 24576); SCHED();
  MA16(ad[0][1], wf0[0], hf[23]); SCHED();
  wf1[0] = TRJ(trlo, TRH(aw22[0] + 24576 + 2048));
  wf1[1] = TRF(aw23[0] + 24576, 2048); SCHED();
  MA16(ad[1][0], wf0[1], hf[22]); MA16(ad[1][1], wf0[1], hf[23]); SCHED(); SCHED();
  POLL_CHECK(pl3, vp3, 4);
  MA16(ad[2][0], wf1[0], hf[22]); SCHED();
  trlo = TRH(aw20[0] + 40960); SCHED();
  MA16(ad[2][1], wf1[0], hf[23]); SCHED();
  wf0[0] = TRJ(trlo, TRH(aw20[0] + 40960 + 2048));
  wf0[1] = TRF(aw21[0] + 40960, 2048);
  BLOAD4(of[0], vh, rWS, so + 0, 0); SCHED();
  MA16(ad[3][0], wf1[1], hf[22]); MA16(ad[3][1], wf1[1], hf[23]); SCHED();
  BLOAD4(of[1], vh, rWS, so + 0, 1024); SCHED();
  asm volatile("s_waitcnt vmcnt(4)" : "+v"(hf[24]), "+v"(hf[25]) :: "memory");
  MA16(ad[0][0], wf0[0], hf[24]); SCHED();
  trlo = TRH(aw22[0] + 40960); SCHED();
  MA16(ad[0][1], wf0[0], hf[25]); SCHED();
  wf1[0] = TRJ(trlo, TRH(aw22[0] + 40960 + 2048));
  wf1[1] = TRF(aw23[0] + 40960, 2048);
  BLOAD4(of[2], vh, rWS, so + 0, 2048); SCHED();
  MA16(ad[1][0], wf0[1], hf[24]); MA16(ad[1][1], wf0[1], hf[25]); SCHED();
  BLOAD4(of[3], vh, rWS, so + 0, 3072); SCHED();
  MA16(ad[2][0], wf1[0], hf[24]); SCHED();
  trlo = TRH(aw20[1] + 57344); SCHED();
  MA16(ad[2][1], wf1[0], hf[25]); SCHED();
  wf0[0] = TRJ(trlo, TRH(aw20[1] + 57344 + 2048));
  wf0[1] = TRF(aw21[1] + 57344, 2048);
  BLOAD4(of[4], vh, rWS, so + 4096, 0); SCHED();
  MA16(ad[3][0], wf1[1], hf[24]); MA16(ad[3][1], wf1[1], hf[25]); SCHED(); SCHED();
  asm volatile("s_waitcnt vmcnt(5)" : "+v"(hf[26]), "+v"(hf[27]) :: "memory");
  ad[0][0] = MFMA16(wf0[0], hf[26], ad[0][0]); ad[0][1] = MFMA16(wf0[0], hf[27], ad[0][1]); asm volatile("" : "+v"(ad[0][0]), "+v"(ad[0][1])); SCHED();
  wf1[0] = TRF(aw22[1] + 57344, 2048);
  wf1[1] = TRF(aw23[1] + 57344, 2048);
  BLOAD4(of[5], vh, rWS, so + 4096, 1024); SCHED();
  ad[1][0] = MFMA16(wf0[1], hf[26], ad[1][0]); ad[1][1] = MFMA16(wf0[1], hf[27], ad[1][1]); asm volatile("" : "+v"(ad[1][0]), "+v"(ad[1][1])); SCHED(); SCHED();
  ad[2][0] = MFMA16(wf1[0], hf[26], ad[2][0]); ad[2][1] = MFMA16(wf1[0], hf[27], ad[2][1]); asm volatile("" : "+v"(ad[2][0]), "+v"(ad[2][1])); SCHED();
  BLOAD4(of[6], vh, rWS, so + 4096, 2048); SCHED();
  ad[3][0] = MFMA16(wf1[1], hf[26], ad[3][0]); ad[3][1] = MFMA16(wf1[1], hf[27], ad[3][1]); asm volatile("" : "+v"(ad[3][0]), "+v"(ad[3][1])); SCHED(); SCHED();
  asm volatile("s_waitcnt vmcnt(5)" : "+v"(of[0]), "+v"(of[1]) :: "memory");
  MZ16(as[0][0], wo_[0][0], of[0]); MZ16(as[0][1], wo_[0][0], of[1]); SCHED();
  P2_WR(ad, 0, 0);
  BLOAD4(of[7], vh, rWS, so + 4096, 3072); SCHED();
  MZ16(as[1][0], wo_[0][1], of[0]); MZ16(as[1][1], wo_[0][1], of[1]); SCHED();
  P2_WR(ad, 0, 1); SCHED();
  MZ16(as[2][0], wo_[1][0], of[0]); MZ16(as[2][1], wo_[1][0], of[1]); SCHED();
  P2_WR(ad, 0, 2); SCHED();
  MZ16(as[3][0], wo_[1][1], of[0]); MZ16(as[3][1], wo_[1][1], of[1]); SCHED();
  P2_WR(ad, 0, 3); SCHED();
  asm volatile("s_waitcnt lgkmcnt(0)\n\ts_barrier" :  :: "memory");
  P2_READ(0); SCHED();
  asm volatile("s_waitcnt vmcnt(4)" : "+v"(of[2]), "+v"(of[3]) :: "memory");
  MA16(as[0][0], wo_[2][0], of[2]); MA16(as[0][1], wo_[2][0], of[3]); SCHED();
  MA16(as[1][0], wo_[2][1], of[2]); MA16(as[1][1], wo_[2][1], of[3]); SCHED();
  P2_RED(rd); SCHED();
  MA16(as[2][0], wo_[3][0], of[2]); MA16(as[2][1], wo_[3][0], of[3]); SCHED();
  MA16(as[3][0], wo_[3][1], of[2]); MA16(as[3][1], wo_[3][1], of[3]); SCHED();
  P2_S1(0); SCHED();
  asm volatile("s_waitcnt vmcnt(2)" : "+v"(of[4]), "+v"(of[5]) :: "memory");
  MA16(as[0][0], wo_[4][0], of[4]); MA16(as[0][1], wo_[4][0], of[5]); SCHED();
  P2_S1(4); SCHED();
  MA16(as[1][0], wo_[4][1], of[4]); MA16(as[1][1], wo_[4][1], of[5]); SCHED();
  P2_S2(0); SCHED();
  MA16(as[2][0], wo_[5][0], of[4]); MA16(as[2][1], wo_[5][0], of[5]); SCHED();
  P2_S2(4); SCHED();
  MA16(as[3][0], wo_[5][1], of[4]); MA16(as[3][1], wo_[5][1], of[5]); SCHED();
  P2_S3(); SCHED();
  asm volatile("s_waitcnt vmcnt(0)" : "+v"(of[6]), "+v"(of[7]) :: "memory");
  as[0][0] = MFMA16(wo_[6][0], of[6], as[0][0]); as[0][1] = MFMA16(wo_[6][0], of[7], as[0][1]); asm volatile("" : "+v"(as[0][0]), "+v"(as[0][1])); SCHED();
  as[1][0] = MFMA16(wo_[6][1], of[6], as[1][0]); as[1][1] = MFMA16(wo_[6][1], of[7], as[1][1]); asm volatile("" : "+v"(as[1][0]), "+v"(as[1][1])); SCHED();
  as[2][0] = MFMA16(wo_[7][0], of[6], as[2][0]); as[2][1] = MFMA16(wo_[7][0], of[7], as[2][1]); asm volatile("" : "+v"(as[2][0]), "+v"(as[2][1])); SCHED();
  P2_WR(as, 40960, 0); SCHED();
  as[3][0] = MFMA16(wo_[7][1], of[6], as[3][0]); as[3][1] = MFMA16(wo_[7][1], of[7], as[3][1]); asm volatile("" : "+v"(as[3][0]), "+v"(as[3][1])); SCHED();
  P2_WR(as, 40960, 1); SCHED();
  P2_WR(as, 40960, 2); P2_WR(as, 40960, 3);
  asm volatile("s_waitcnt lgkmcnt(0)\n\ts_barrier" :  :: "memory");
  P2_READ(40960); SCHED();
  P2_RED(rs);
  P2_FINAL();
  } else {
    const u32 a = quad & 1, g = 8 * half + 4 * (quad >> 1) + li, h = 8 * a + w;
    const u32 arot = (7 * li) >> 2;
    const u32 tidx = li + 4 * half, tn = 7 * quad + (tidx < 7 ? tidx : 6), tj = tn >> 1, tb = tn & 1;
    const u32 vc0 = r8 * 2688 + c8 * 16, sc0 = tb * 256 * 2688 + (448 + 64 * tj) * 2;
    const u32 vc1 = r8 * 3584 + c8 * 16, sc1 = 256 * tj;
    const u32 vwq = r8 * 2688 + ((c8 ^ (((r8 >> 1) & 1) << 2)) << 4);
    const u32 sx = g * 128 * 896, swq = 384 * a;
    const u32 i16 = lane & 15, qd = lane >> 4;
    u32x4 sv[7], cv0[4], cv1[7], cv2[7], xq0[4], xq1[4], wq0, wq1, kf[5][2], vf[5][2], qf[8], cmask;
    f32x16 aq[4]; f32x4 acs;
    u32x4 gr, ov_[8]; u32 pl, nmq[4]; u32x2 pp_[8], pc_[8];
    float cq[8], mean; f32x2 vs;
    const u32 vsx = vfirst, ssx = sfirst;
    const __amdgpu_buffer_rsrc_t rC = __builtin_amdgcn_make_buffer_rsrc((void*)(tb ? wu : wg), 0, D_ * F_ * 2, 0x00020000);
    f32x4 cprev, ccur; u32 mc0, mc1;
    u32 ax0[2], ax1[2], ax2[2], ax3[2], aw0[2], vqs, vp1, vk0, vk1, vv0, vv1, sk, skh, sv_, svh, vfo, vos;
    u32x4 csel;
    for (int e = 0; e < 4; ++e) csel[e] = (i16 == 2 * e ? 0x3f80u : 0u) | (i16 == 2 * e + 1 ? 0x3f800000u : 0u);
#define ATT_STATS_A() do { \
      float s0 = 0.f, s1 = 0.f; \
      for (int i = 0; i < 7; ++i) for (int e = 0; e < 4; e += 2) { s0 = DOT2(sv[i][e], 0x3f803f80u, s0); s1 = DOT2(sv[i][e + 1], 0x3f803f80u, s1); } \
      mean = red8_add(s0 + s1) * (1.0f / 448.0f); vs = f32x2{0.f, 0.f}; asm volatile("" : "+v"(mean)); \
    } while (0)
#define ATT_STATS_B(I0, I1) do { \
      for (int i = (I0); i < (I1); ++i) for (int e = 0; e < 4; ++e) { const f32x2 d_ = f32x2{BLO(sv[i][e]), BHI(sv[i][e])} - mean; vs += d_ * d_; } \
      asm volatile("" : "+v"(vs)); } while (0)
#define ATT_STATS_C() do { \
      const float var = RBF(red8_add(vs[0] + vs[1]) * (1.0f / 448.0f)); \
      const float den = __builtin_amdgcn_sqrtf(RBF(var + 1e-5f)); \
      ST2((u32x2{PK(mean, den), ep}), c8 == 0 ? WS_STAT + (128 * g + 64 * a + r8) * 8 : 0x7ffffff0u); \
    } while (0)
#define CS_PART(CS, C) do { \
      for (int e = 0; e < 4; ++e) CS[e] += dpp<0x128>(CS[e]); \
      if (lane < 32 && !(lane & 8)) *(LAS f32x4*)(lb + 18 * 8192 + (C) * 2048 + w * 256 + (8 * c8 + 4 * qd) * 4) = CS; \
    } while (0)
#define CS_SUM(C) ({ float t_ = 0.f; for (int k = 0; k < 8; ++k) t_ += *(LAS float*)(lb + 18 * 8192 + (C) * 2048 + k * 256 + lane * 4); t_; })
#define CS_FIN(C) do { if (w == 1 + (C) && tidx < 7) { const float t0_ = CS_SUM(C); \
      ST2((u32x2{__float_as_uint(t0_), ep}), WS_CS + (3 * tj + (C)) * 1024 + lane * 16 + tb * 8); } } while (0)
#define ATT_SETUP() do { \
      const u32 i32 = lane & 31, hh = lane >> 5, xb = lb + i32 * 128, xs = (i32 >> 1) & 7; \
      ax0[0] = xb + (((0 + hh) ^ xs) << 4); ax1[0] = xb + (((2 + hh) ^ xs) << 4); ax2[0] = xb + (((4 + hh) ^ xs) << 4); ax3[0] = xb + (((6 + hh) ^ xs) << 4); \
      WIN(ax0, 2); WIN(ax1, 2); WIN(ax2, 2); WIN(ax3, 2); \
      const u32 q_ = i16 >> 2, p_ = lane & 3, g4 = 4 * (qd & 1) + p_, l0 = 28 * h - 192 * a + 4 * (g4 == 7 ? 6 : g4); \
      aw0[0] = lb + (l0 >> 6) * 8192 + (8 * hh + q_) * 128 + (((l0 & 63) * 2) ^ ((q_ >> 1) << 6)); WIN(aw0, 2); \
      { const u32 on_ = ((qd ^ q_) & 1) ? 0u : 0x3f803f80u; cmask = u32x4{on_, on_, on_, on_}; } \
      vqs = WS_STAT + g * 1024 + lane * 16; asm volatile("" : "+v"(cmask)); \
      { const u32 col = 28 * h + ((lane & 1) ? 27 : 0) + ((lane & 2) ? 448 : 0), gg = ((lane & 4) && g > 0) ? g - 1 : g; \
        vp1 = WS_FL + (gg * 14 + (col >> 6)) * 128; } \
      vk0 = (7 * h + 2 * (qd & 1) + (qd >> 1)) * 256 + i16 * 16; vk1 = qd == 3 ? 0x7ffffff0u - WS_END : vk0 + 1024;     \
      vv0 = qd * 8192 + h * 512 + i16 * 16; vv1 = i16 < 12 ? vv0 + 256 : 0x7ffffff0u - WS_END; \
      sk = WS_K + 4 * g * 28672; skh = g == 0 ? sk : sk - 28672; sv_ = WS_V + 4 * g * 32768; svh = g == 0 ? sv_ : sv_ - 32768; \
      vfo = WS_FO + (g * 16 + h) * 128; \
      vos = WS_O + 4 * g * 32768 + h * 2048 + lane * 16; \
    } while (0)
#define ATT_QS() do { float qe[4], qo[4]; bcast4(__uint_as_float(gr.x), qe); bcast4(__uint_as_float(gr.z), qo); \
      for (int b = 0; b < 4; ++b) { \
        cq[2 * b] = (0.18898223650461362f * 1.4426950408889634f) * __builtin_amdgcn_rcpf(BHI(__float_as_uint(qe[b]))); \
        cq[2 * b + 1] = (0.18898223650461362f * 1.4426950408889634f) * __builtin_amdgcn_rcpf(BHI(__float_as_uint(qo[b]))); \
        nmq[b] = (__float_as_uint((qd & 1) ? qo[b] : qe[b]) & 0xffffu) ^ 0x8000u; \
        asm volatile("" : "+v"(cq[2 * b]), "+v"(cq[2 * b + 1]), "+v"(nmq[b])); } \
      bool vc[4]; \
      for (int r = 0; r < 4; ++r) { const int ky = 4 * qd + r; vc[r] = (u32)((int)i16 - ky) < 8u; \
        ccur[r] = vc[r] ? 0.f : -__builtin_inff(); cprev[r] = ky >= (int)i16 + 9 ? 0.f : -__builtin_inff(); } \
      mc0 = (vc[0] ? 0xffffu : 0u) | (vc[1] ? 0xffff0000u : 0u); mc1 = (vc[2] ? 0xffffu : 0u) | (vc[3] ? 0xffff0000u : 0u); \
      asm volatile("" : "+v"(ccur), "+v"(cprev), "+v"(mc0), "+v"(mc1)); } while (0)
#define ATT_QCORR() do { const u32x4 cf_ = CSFRAG(qd >= 2 ? 0u : __float_as_uint(acs[0])); \
      for (int b = 0; b < 4; ++b) { aq[b] = MFMA32(cf_, u32x4{nmq[b] | (nmq[b] << 16), nmq[b], 0u, 0u}, aq[b]); asm volatile("" : "+v"(aq[b])); } } while (0)
#define ATT_QEPI(B) do { u32 k_[8]; \
      for (int e = 0; e < 8; ++e) k_[e] = PK(aq[B][2 * e], aq[B][2 * e + 1]); \
      for (int t_ = 0; t_ < 2; ++t_) for (int d_ = 0; d_ < 2; ++d_) { \
        auto r_ = __builtin_amdgcn_permlane16_swap(k_[4 * t_ + d_], k_[4 * t_ + 2 + d_], false, false); \
        qf[2 * (B)][2 * t_ + d_] = r_[0]; qf[2 * (B) + 1][2 * t_ + d_] = r_[1]; } \
      asm volatile("" : "+v"(qf[2 * (B)]), "+v"(qf[2 * (B) + 1])); } while (0)
#define HALF(V, P) u32x2{V[2 * (P)], V[2 * (P) + 1]}
#define ATT_SM(HB) do { \
      float vl[4][4], mx[4], sm[4]; \
      for (int k = 0; k < 4; ++k) { const int bl = 2 * (HB) + 1 + (k >> 1), pa = k & 1, rt_ = 4 * (HB) + k; \
        f32x4 cp = cprev; \
        if ((HB) == 0 && k < 2 && g == 0) cp = f32x4{-__builtin_inff(), -__builtin_inff(), -__builtin_inff(), -__builtin_inff()}; \
        const f32x4 sp = MFMA16H(HALF(kf[bl - 1][1], pa), HALF(qf[rt_], 1), MFMA16H(HALF(kf[bl - 1][0], pa), HALF(qf[rt_], 0), cp)); \
        const f32x4 sc = MFMA16H(HALF(kf[bl][1], pa), HALF(qf[rt_], 1), MFMA16H(HALF(kf[bl][0], pa), HALF(qf[rt_], 0), ccur)); \
        for (int r = 0; r < 4; ++r) vl[k][r] = FMAX(sp[r], sc[r]); \
        mx[k] = FMAX(FMAX(FMAX(vl[k][0], vl[k][1]), vl[k][2]), vl[k][3]); } \
      { const float z = sw32_max(sw16_max(mx[0], mx[1]), sw16_max(mx[2], mx[3])); bcast4(z, mx); } \
      for (int k = 0; k < 4; ++k) { const float c_ = cq[4 * (HB) + k], mc_ = -mx[k] * c_; \
        for (int r = 0; r < 4; ++r) vl[k][r] = __builtin_amdgcn_exp2f(__builtin_fmaf(vl[k][r], c_, mc_)); \
        sm[k] = (vl[k][0] + vl[k][1]) + (vl[k][2] + vl[k][3]); } \
      { const float z = __builtin_amdgcn_rcpf(sw32_add(sw16_add(sm[0], sm[1]), sw16_add(sm[2], sm[3]))); bcast4(z, sm); } \
      for (int k = 0; k < 4; ++k) { \
        const u32 p01 = PK(vl[k][0] * sm[k], vl[k][1] * sm[k]), p23 = PK(vl[k][2] * sm[k], vl[k][3] * sm[k]); \
        pp_[4 * (HB) + k] = u32x2{p01 & ~mc0, p23 & ~mc1}; pc_[4 * (HB) + k] = u32x2{p01 & mc0, p23 & mc1}; asm volatile("" : "+v"(pp_[4 * (HB) + k]), "+v"(pc_[4 * (HB) + k])); } \
    } while (0)
#define ATT_PV(HB) do { \
      const f32x4 z4 = {0.f, 0.f, 0.f, 0.f}; \
      for (int k = 0; k < 4; ++k) { const int bl = 2 * (HB) + 1 + (k >> 1), pa = k & 1; \
        const f32x4 o0 = MFMA16H(HALF(vf[bl][0], pa), pc_[4 * (HB) + k], MFMA16H(HALF(vf[bl - 1][0], pa), pp_[4 * (HB) + k], z4)); \
        const f32x4 o1 = MFMA16H(HALF(vf[bl][1], pa), pc_[4 * (HB) + k], MFMA16H(HALF(vf[bl - 1][1], pa), pp_[4 * (HB) + k], z4)); \
        ov_[4 * (HB) + k] = u32x4{PK(o0[0], o0[1]), PK(o0[2], o0[3]), PK(o1[0], o1[1]), PK(o1[2], o1[3])}; \
        asm volatile("" : "+v"(ov_[4 * (HB) + k])); } \
    } while (0)
#define ATT_OST(HB) do { for (int k = 4 * (HB); k < 4 * (HB) + 4; ++k) ST4(ov_[k], vos + (k >> 1) * 32768 + (k & 1) * 1024); } while (0)
  BLOAD4P(sv[0], vsx, rX, ssx, 0);
  BLOAD4P(sv[1], vsx, rX, ssx, 128);
  BLOAD4P(sv[2], vsx, rX, ssx, 256);
  BLOAD4P(sv[3], vsx, rX, ssx, 384);
  BLOAD4P(sv[4], vsx, rX, ssx, 512);
  BLOAD4P(sv[5], vsx, rX, ssx, 640);
  BLOAD4P(sv[6], vsx, rX, ssx, 768);
  EPOCH_ISSUE();
  BLOAD4P(cv0[0], vc0, rQKV, sc0 + 0, 0);
  BLOAD4P(cv0[1], vc0, rQKV, sc0 + 172032, 0);
  BLOAD4P(cv0[2], vc0, rQKV, sc0 + 344064, 0);
  BLOAD4P(cv0[3], vc0, rQKV, sc0 + 516096, 0);
  EPOCH_LOAD();
  BLOAD4P(cv1[0], vc1, rC, sc1 + 0, 0); SCHED();
  asm volatile("s_waitcnt vmcnt(5)" : "+v"(sv[0]), "+v"(sv[1]), "+v"(sv[2]), "+v"(sv[3]), "+v"(sv[4]), "+v"(sv[5]), "+v"(sv[6]) :: "memory");
  ATT_STATS_A();
  BLOAD4P(cv1[1], vc1, rC, sc1 + 229376, 0); SCHED();
  ATT_STATS_B(0, 2);
  BLOAD4P(cv1[2], vc1, rC, sc1 + 458752, 0); SCHED();
  ATT_STATS_B(2, 4);
  BLOAD4P(cv1[3], vc1, rC, sc1 + 688128, 0); SCHED();
  ATT_STATS_B(4, 6);
  BLOAD4P(cv1[4], vc1, rC, sc1 + 917504, 0); SCHED();
  ATT_STATS_B(6, 7);
  BLOAD4P(cv1[5], vc1, rC, sc1 + 1146880, 0); SCHED();
  ATT_STATS_C();
  asm volatile("s_waitcnt vmcnt(7)" : "+v"(cv0[0]), "+v"(cv0[1]), "+v"(cv0[2]), "+v"(cv0[3]) :: "memory");
  { f32x4 cs = {0.f, 0.f, 0.f, 0.f}; for (int i = 0; i < 4; ++i) cs = MFMA16(csel, cv0[i], cs); CS_PART(cs, 0); }
  BLOAD4P(cv1[6], vc1, rC, sc1 + 1376256, 0); SCHED();
  asm volatile("s_waitcnt lgkmcnt(0)\n\ts_barrier" :  :: "memory");
  CS_FIN(0);
  GLDS(0, vx, rX, sx + KA(0)*128 + 0);
  GLDS(8192, vx, rX, sx + KA(0)*128 + 57344);
  GLDS(16384, vwq, rQKV, swq + KA(0)*172032 + 0);
  GLDS(24576, vwq, rQKV, swq + KA(0)*172032 + 128); SCHED();
  ATT_SETUP();
  GLDS(32768, vwq, rQKV, swq + KA(0)*172032 + 256);
  GLDS(40960, vwq, rQKV, swq + KA(0)*172032 + 384); SCHED();
  asm volatile("s_waitcnt vmcnt(6)" : "+v"(cv1[0]), "+v"(cv1[1]), "+v"(cv1[2]), "+v"(cv1[3]), "+v"(cv1[4]), "+v"(cv1[5]), "+v"(cv1[6]) :: "memory");
  { f32x4 cs = {0.f, 0.f, 0.f, 0.f}; for (int i = 0; i < 7; ++i) cs = MFMA16(csel, cv1[i], cs); CS_PART(cs, 1); }
  GLDS(49152, vx, rX, sx + KA(1)*128 + 0);
  GLDS(57344, vx, rX, sx + KA(1)*128 + 57344);
  GLDS(65536, vwq, rQKV, swq + KA(1)*172032 + 0);
  GLDS(73728, vwq, rQKV, swq + KA(1)*172032 + 128);
  GLDS(81920, vwq, rQKV, swq + KA(1)*172032 + 256); SCHED();
  asm volatile("s_waitcnt lgkmcnt(0)\n\ts_waitcnt vmcnt(5)\n\ts_barrier" :  :: "memory");
  wq0 = TRF(aw0[0] + 16384, 512);
  xq0[0] = LDS128(ax0[0] + 0);
  xq0[1] = LDS128(ax0[0] + 4096);
  xq0[2] = LDS128(ax0[0] + 8192);
  xq0[3] = LDS128(ax0[0] + 12288);
  CS_FIN(1); SCHED();
  MZ16(acs, cmask, wq0); MZ32(aq[0], wq0, xq0[0]); SCHED();
  wq1 = TRF(aw0[0] + 18432, 512);
  xq1[0] = LDS128(ax1[0] + 0);
  GLDS(90112, vwq, rQKV, swq + KA(1)*172032 + 384); SCHED();
  MZ32(aq[1], wq0, xq0[1]); SCHED();
  xq1[1] = LDS128(ax1[0] + 4096); SCHED();
  MZ32(aq[2], wq0, xq0[2]); SCHED();
  xq1[2] = LDS128(ax1[0] + 8192);
  GLDS(98304, vx, rX, sx + KA(2)*128 + 0); SCHED();
  MZ32(aq[3], wq0, xq0[3]); SCHED();
  xq1[3] = LDS128(ax1[0] + 12288); SCHED();
  MA16(acs, cmask, wq1); MA32(aq[0], wq1, xq1[0]); SCHED();
  wq0 = TRF(aw0[0] + 20480, 512);
  xq0[0] = LDS128(ax2[0] + 0);
  GLDS(106496, vx, rX, sx + KA(2)*128 + 57344); SCHED();
  MA32(aq[1], wq1, xq1[1]); SCHED();
  xq0[1] = LDS128(ax2[0] + 4096); SCHED();
  MA32(aq[2], wq1, xq1[2]); SCHED();
  xq0[2] = LDS128(ax2[0] + 8192);
  GLDS(114688, vwq, rQKV, swq + KA(2)*172032 + 0); SCHED();
  MA32(aq[3], wq1, xq1[3]); SCHED();
  xq0[3] = LDS128(ax2[0] + 12288); SCHED();
  MA16(acs, cmask, wq0); MA32(aq[0], wq0, xq0[0]); SCHED();
  wq1 = TRF(aw0[0] + 22528, 512);
  xq1[0] = LDS128(ax3[0] + 0);
  xq1[1] = LDS128(ax3[0] + 4096);
  GLDS(122880, vwq, rQKV, swq + KA(2)*172032 + 128); SCHED();
  MA32(aq[1], wq0, xq0[1]); SCHED();
  xq1[2] = LDS128(ax3[0] + 8192);
  xq1[3] = LDS128(ax3[0] + 12288); SCHED();
  MA32(aq[2], wq0, xq0[2]); SCHED();
  GLDS(131072, vwq, rQKV, swq + KA(2)*172032 + 256); SCHED();
  MA32(aq[3], wq0, xq0[3]); SCHED();
  asm volatile("s_waitcnt lgkmcnt(0)\n\ts_waitcnt vmcnt(5)\n\ts_barrier" :  :: "memory");
  MA16(acs, cmask, wq1); MA32(aq[0], wq1, xq1[0]); SCHED();
  wq0 = TRF(aw0[1] + 0, 512);
  xq0[0] = LDS128(ax0[0] + 49152);
  GLDS(139264, vwq, rQKV, swq + KA(2)*172032 + 384); SCHED();
  MA32(aq[1], wq1, xq1[1]); SCHED();
  xq0[1] = LDS128(ax0[0] + 53248); SCHED();
  MA32(aq[2], wq1, xq1[2]); SCHED();
  xq0[2] = LDS128(ax0[0] + 57344);
  GLDS(0, vx, rX, sx + KA(3)*128 + 0); SCHED();
  MA32(aq[3], wq1, xq1[3]); SCHED();
  xq0[3] = LDS128(ax0[0] + 61440);
  BLOAD4P(cv2[0], vc1, rC, sc1 + 0, 128); SCHED();
  MA16(acs, cmask, wq0); MA32(aq[0], wq0, xq0[0]); SCHED();
  wq1 = TRF(aw0[1] + 2048, 512);
  xq1[0] = LDS128(ax1[0] + 49152);
  GLDS(8192, vx, rX, sx + KA(3)*128 + 57344); SCHED();
  MA32(aq[1], wq0, xq0[1]); SCHED();
  xq1[1] = LDS128(ax1[0] + 53248); SCHED();
  MA32(aq[2], wq0, xq0[2]); SCHED();
  xq1[2] = LDS128(ax1[0] + 57344);
  GLDS(16384, vwq, rQKV, swq + KA(3)*172032 + 0); SCHED();
  MA32(aq[3], wq0, xq0[3]); SCHED();
  xq1[3] = LDS128(ax1[0] + 61440);
  BLOAD4P(cv2[1], vc1, rC, sc1 + 229376, 128); SCHED();
  MA16(acs, cmask, wq1); MA32(aq[0], wq1, xq1[0]); SCHED();
  wq0 = TRF(aw0[1] + 4096, 512);
  xq0[0] = LDS128(ax2[0] + 49152);
  GLDS(24576, vwq, rQKV, swq + KA(3)*172032 + 128); SCHED();
  MA32(aq[1], wq1, xq1[1]); SCHED();
  xq0[1] = LDS128(ax2[0] + 53248); SCHED();
  MA32(aq[2], wq1, xq1[2]); SCHED();
  xq0[2] = LDS128(ax2[0] + 57344);
  GLDS(32768, vwq, rQKV, swq + KA(3)*172032 + 256); SCHED();
  MA32(aq[3], wq1, xq1[3]); SCHED();
  xq0[3] = LDS128(ax2[0] + 61440);
  BLOAD4P(cv2[2], vc1, rC, sc1 + 458752, 128); SCHED();
  MA16(acs, cmask, wq0); MA32(aq[0], wq0, xq0[0]); SCHED();
  wq1 = TRF(aw0[1] + 6144, 512);
  xq1[0] = LDS128(ax3[0] + 49152);
  xq1[1] = LDS128(ax3[0] + 53248);
  GLDS(40960, vwq, rQKV, swq + KA(3)*172032 + 384); SCHED();
  MA32(aq[1], wq0, xq0[1]); SCHED();
  xq1[2] = LDS128(ax3[0] + 57344);
  xq1[3] = LDS128(ax3[0] + 61440); SCHED();
  MA32(aq[2], wq0, xq0[2]); SCHED(); SCHED();
  MA32(aq[3], wq0, xq0[3]); SCHED();
  BLOAD4P(cv2[3], vc1, rC, sc1 + 688128, 128); SCHED();
  asm volatile("s_waitcnt lgkmcnt(0)\n\ts_waitcnt vmcnt(10)\n\ts_barrier" :  :: "memory");
  MA16(acs, cmask, wq1); MA32(aq[0], wq1, xq1[0]); SCHED();
  wq0 = TRF(aw0[1] + 49152, 512);
  xq0[0] = LDS128(ax0[1] + 32768);
  GLDS(49152, vx, rX, sx + KA(4)*128 + 0); SCHED();
  MA32(aq[1], wq1, xq1[1]); SCHED();
  xq0[1] = LDS128(ax0[1] + 36864); SCHED();
  MA32(aq[2], wq1, xq1[2]); SCHED();
  xq0[2] = LDS128(ax0[1] + 40960);
  GLDS(57344, vx, rX, sx + KA(4)*128 + 57344); SCHED();
  MA32(aq[3], wq1, xq1[3]); SCHED();
  xq0[3] = LDS128(ax0[1] + 45056);
  BLOAD4P(cv2[4], vc1, rC, sc1 + 917504, 128); SCHED();
  MA16(acs, cmask, wq0); MA32(aq[0], wq0, xq0[0]); SCHED();
  wq1 = TRF(aw0[1] + 51200, 512);
  xq1[0] = LDS128(ax1[1] + 32768);
  GLDS(65536, vwq, rQKV, swq + KA(4)*172032 + 0); SCHED();
  MA32(aq[1], wq0, xq0[1]); SCHED();
  xq1[1] = LDS128(ax1[1] + 36864); SCHED();
  MA32(aq[2], wq0, xq0[2]); SCHED();
  xq1[2] = LDS128(ax1[1] + 40960);
  GLDS(73728, vwq, rQKV, swq + KA(4)*172032 + 128); SCHED();
  MA32(aq[3], wq0, xq0[3]); SCHED();
  xq1[3] = LDS128(ax1[1] + 45056);
  BLOAD4P(cv2[5], vc1, rC, sc1 + 1146880, 128); SCHED();
  MA16(acs, cmask, wq1); MA32(aq[0], wq1, xq1[0]); SCHED();
  wq0 = TRF(aw0[1] + 53248, 512);
  xq0[0] = LDS128(ax2[1] + 32768);
  GLDS(81920, vwq, rQKV, swq + KA(4)*172032 + 256); SCHED();
  MA32(aq[1], wq1, xq1[1]); SCHED();
  xq0[1] = LDS128(ax2[1] + 36864); SCHED();
  MA32(aq[2], wq1, xq1[2]); SCHED();
  xq0[2] = LDS128(ax2[1] + 40960);
  GLDS(90112, vwq, rQKV, swq + KA(4)*172032 + 384); SCHED();
  MA32(aq[3], wq1, xq1[3]); SCHED();
  xq0[3] = LDS128(ax2[1] + 45056);
  BLOAD4P(cv2[6], vc1, rC, sc1 + 1376256, 128); SCHED();
  MA16(acs, cmask, wq0); MA32(aq[0], wq0, xq0[0]); SCHED();
  wq1 = TRF(aw0[1] + 55296, 512);
  xq1[0] = LDS128(ax3[1] + 32768);
  xq1[1] = LDS128(ax3[1] + 36864); SCHED();
  MA32(aq[1], wq0, xq0[1]); SCHED();
  xq1[2] = LDS128(ax3[1] + 40960);
  xq1[3] = LDS128(ax3[1] + 45056); SCHED();
  MA32(aq[2], wq0, xq0[2]); SCHED(); SCHED();
  MA32(aq[3], wq0, xq0[3]); SCHED();
  asm volatile("s_waitcnt lgkmcnt(0)\n\ts_waitcnt vmcnt(10)\n\ts_barrier" :  :: "memory");
  MA16(acs, cmask, wq1); MA32(aq[0], wq1, xq1[0]); SCHED();
  wq0 = TRF(aw0[0] + 16384, 512);
  xq0[0] = LDS128(ax0[0] + 0);
  GLDS(98304, vx, rX, sx + KA(5)*128 + 0); SCHED();
  MA32(aq[1], wq1, xq1[1]); SCHED();
  xq0[1] = LDS128(ax0[0] + 4096);
  BLOAD4(gr, vqs, rWS, 0, 0); SCHED();
  MA32(aq[2], wq1, xq1[2]); SCHED();
  xq0[2] = LDS128(ax0[0] + 8192);
  GLDS(106496, vx, rX, sx + KA(5)*128 + 57344); SCHED();
  MA32(aq[3], wq1, xq1[3]); SCHED();
  xq0[3] = LDS128(ax0[0] + 12288); SCHED();
  MA16(acs, cmask, wq0); MA32(aq[0], wq0, xq0[0]); SCHED();
  wq1 = TRF(aw0[0] + 18432, 512);
  xq1[0] = LDS128(ax1[0] + 0);
  GLDS(114688, vwq, rQKV, swq + KA(5)*172032 + 0); SCHED();
  MA32(aq[1], wq0, xq0[1]); SCHED();
  xq1[1] = LDS128(ax1[0] + 4096); SCHED();
  MA32(aq[2], wq0, xq0[2]); SCHED();
  xq1[2] = LDS128(ax1[0] + 8192);
  GLDS(122880, vwq, rQKV, swq + KA(5)*172032 + 128); SCHED();
  MA32(aq[3], wq0, xq0[3]); SCHED();
  xq1[3] = LDS128(ax1[0] + 12288); SCHED();
  MA16(acs, cmask, wq1); MA32(aq[0], wq1, xq1[0]); SCHED();
  wq0 = TRF(aw0[0] + 20480, 512);
  xq0[0] = LDS128(ax2[0] + 0);
  GLDS(131072, vwq, rQKV, swq + KA(5)*172032 + 256); SCHED();
  MA32(aq[1], wq1, xq1[1]); SCHED();
  xq0[1] = LDS128(ax2[0] + 4096); SCHED();
  MA32(aq[2], wq1, xq1[2]); SCHED();
  xq0[2] = LDS128(ax2[0] + 8192);
  GLDS(139264, vwq, rQKV, swq + KA(5)*172032 + 384); SCHED();
  MA32(aq[3], wq1, xq1[3]); SCHED();
  xq0[3] = LDS128(ax2[0] + 12288); SCHED();
  MA16(acs, cmask, wq0); MA32(aq[0], wq0, xq0[0]); SCHED();
  wq1 = TRF(aw0[0] + 22528, 512);
  xq1[0] = LDS128(ax3[0] + 0);
  xq1[1] = LDS128(ax3[0] + 4096); SCHED();
  MA32(aq[1], wq0, xq0[1]); SCHED();
  xq1[2] = LDS128(ax3[0] + 8192);
  xq1[3] = LDS128(ax3[0] + 12288);
  asm volatile("s_waitcnt vmcnt(7)" : "+v"(cv2[0]), "+v"(cv2[1]), "+v"(cv2[2]), "+v"(cv2[3]), "+v"(cv2[4]), "+v"(cv2[5]), "+v"(cv2[6]) :: "memory");
  { f32x4 cs = {0.f, 0.f, 0.f, 0.f}; for (int i = 0; i < 7; ++i) cs = MFMA16(csel, cv2[i], cs); CS_PART(cs, 2); } SCHED();
  MA32(aq[2], wq0, xq0[2]); SCHED(); SCHED();
  MA32(aq[3], wq0, xq0[3]); SCHED();
  asm volatile("s_waitcnt lgkmcnt(0)\n\ts_barrier" :  :: "memory");
  MA16(acs, cmask, wq1); MA32(aq[0], wq1, xq1[0]); SCHED();
  wq0 = TRF(aw0[1] + 0, 512);
  xq0[0] = LDS128(ax0[0] + 49152);
  GLDS(0, vx, rX, sx + KA(6)*128 + 0); SCHED();
  MA32(aq[1], wq1, xq1[1]); SCHED();
  xq0[1] = LDS128(ax0[0] + 53248);
  BLOAD1(pl, vp1); SCHED();
  MA32(aq[2], wq1, xq1[2]); SCHED();
  xq0[2] = LDS128(ax0[0] + 57344);
  GLDS(8192, vx, rX, sx + KA(6)*128 + 57344); SCHED();
  MA32(aq[3], wq1, xq1[3]); SCHED();
  xq0[3] = LDS128(ax0[0] + 61440);
  CS_FIN(2);
  GRAN_CHECK(vqs, 0, 8);
  ATT_QS(); SCHED();
  MA16(acs, cmask, wq0); MA32(aq[0], wq0, xq0[0]); SCHED();
  wq1 = TRF(aw0[1] + 2048, 512);
  xq1[0] = LDS128(ax1[0] + 49152);
  GLDS(16384, vwq, rQKV, swq + KA(6)*172032 + 0); SCHED();
  MA32(aq[1], wq0, xq0[1]); SCHED();
  xq1[1] = LDS128(ax1[0] + 53248); SCHED();
  MA32(aq[2], wq0, xq0[2]); SCHED();
  xq1[2] = LDS128(ax1[0] + 57344);
  GLDS(24576, vwq, rQKV, swq + KA(6)*172032 + 128); SCHED();
  MA32(aq[3], wq0, xq0[3]); SCHED();
  xq1[3] = LDS128(ax1[0] + 61440); SCHED();
  MA16(acs, cmask, wq1); MA32(aq[0], wq1, xq1[0]); SCHED();
  wq0 = TRF(aw0[1] + 4096, 512);
  xq0[0] = LDS128(ax2[0] + 49152);
  GLDS(32768, vwq, rQKV, swq + KA(6)*172032 + 256); SCHED();
  MA32(aq[1], wq1, xq1[1]); SCHED();
  xq0[1] = LDS128(ax2[0] + 53248); SCHED();
  MA32(aq[2], wq1, xq1[2]); SCHED();
  xq0[2] = LDS128(ax2[0] + 57344);
  GLDS(40960, vwq, rQKV, swq + KA(6)*172032 + 384); SCHED();
  MA32(aq[3], wq1, xq1[3]); SCHED();
  xq0[3] = LDS128(ax2[0] + 61440); SCHED();
  MA16(acs, cmask, wq0); MA32(aq[0], wq0, xq0[0]); SCHED();
  POLL_CHECK(pl, vp1, 5);
  wq1 = TRF(aw0[1] + 6144, 512);
  xq1[0] = LDS128(ax3[0] + 49152);
  xq1[1] = LDS128(ax3[0] + 53248);
  BLOAD4(kf[0][0], vk0, rWS, skh, 0); SCHED();
  MA32(aq[1], wq0, xq0[1]); SCHED();
  xq1[2] = LDS128(ax3[0] + 57344);
  xq1[3] = LDS128(ax3[0] + 61440); SCHED();
  MA32(aq[2], wq0, xq0[2]); SCHED();
  BLOAD4(kf[0][1], vk1, rWS, skh, 0); SCHED();
  MA32(aq[3], wq0, xq0[3]); SCHED();
  asm volatile("s_waitcnt lgkmcnt(0)\n\ts_barrier" :  :: "memory");
  MA16(acs, cmask, wq1); MA32(aq[0], wq1, xq1[0]); SCHED();
  wq0 = TRF(aw0[1] + 49152, 512);
  xq0[0] = LDS128(ax0[1] + 32768);
  BLOAD4(kf[1][0], vk0, rWS, sk + 0, 0); SCHED();
  MA32(aq[1], wq1, xq1[1]); SCHED();
  xq0[1] = LDS128(ax0[1] + 36864); SCHED();
  MA32(aq[2], wq1, xq1[2]); SCHED();
  xq0[2] = LDS128(ax0[1] + 40960);
  BLOAD4(kf[1][1], vk1, rWS, sk + 0, 0); SCHED();
  MA32(aq[3], wq1, xq1[3]); SCHED();
  xq0[3] = LDS128(ax0[1] + 45056); SCHED();
  MA16(acs, cmask, wq0); MA32(aq[0], wq0, xq0[0]); SCHED();
  wq1 = TRF(aw0[1] + 51200, 512);
  xq1[0] = LDS128(ax1[1] + 32768);
  BLOAD4(kf[2][0], vk0, rWS, sk + 28672, 0); SCHED();
  MA32(aq[1], wq0, xq0[1]); SCHED();
  xq1[1] = LDS128(ax1[1] + 36864); SCHED();
  MA32(aq[2], wq0, xq0[2]); SCHED();
  xq1[2] = LDS128(ax1[1] + 40960);
  BLOAD4(kf[2][1], vk1, rWS, sk + 28672, 0); SCHED();
  MA32(aq[3], wq0, xq0[3]); SCHED();
  xq1[3] = LDS128(ax1[1] + 45056); SCHED();
  MA16(acs, cmask, wq1); MA32(aq[0], wq1, xq1[0]); SCHED();
  wq0 = TRF(aw0[1] + 53248, 512);
  xq0[0] = LDS128(ax2[1] + 32768);
  BLOAD4(vf[0][0], vv0, rWS, svh, 0); SCHED();
  MA32(aq[1], wq1, xq1[1]); SCHED();
  xq0[1] = LDS128(ax2[1] + 36864); SCHED();
  MA32(aq[2], wq1, xq1[2]); SCHED();
  xq0[2] = LDS128(ax2[1] + 40960);
  BLOAD4(vf[0][1], vv1, rWS, svh, 0); SCHED();
  MA32(aq[3], wq1, xq1[3]); SCHED();
  xq0[3] = LDS128(ax2[1] + 45056); SCHED();
  MA16(acs, cmask, wq0); MA32(aq[0], wq0, xq0[0]); SCHED();
  wq1 = TRF(aw0[1] + 55296, 512);
  xq1[0] = LDS128(ax3[1] + 32768);
  xq1[1] = LDS128(ax3[1] + 36864);
  BLOAD4(vf[1][0], vv0, rWS, sv_ + 0, 0); SCHED();
  MA32(aq[1], wq0, xq0[1]); SCHED();
  xq1[2] = LDS128(ax3[1] + 40960);
  xq1[3] = LDS128(ax3[1] + 45056); SCHED();
  MA32(aq[2], wq0, xq0[2]); SCHED();
  BLOAD4(vf[1][1], vv1, rWS, sv_ + 0, 0); SCHED();
  MA32(aq[3], wq0, xq0[3]); SCHED();
  asm volatile("s_waitcnt lgkmcnt(0)\n\ts_waitcnt vmcnt(10)\n\ts_barrier" :  :: "memory");
  MA16(acs, cmask, wq1); MA32(aq[0], wq1, xq1[0]); SCHED();
  wq0 = TRF(aw0[0] + 16384, 512);
  xq0[0] = LDS128(ax0[0] + 0);
  BLOAD4(vf[2][0], vv0, rWS, sv_ + 32768, 0); SCHED();
  MA32(aq[1], wq1, xq1[1]); SCHED();
  xq0[1] = LDS128(ax0[0] + 4096); SCHED();
  MA32(aq[2], wq1, xq1[2]); SCHED();
  xq0[2] = LDS128(ax0[0] + 8192);
  BLOAD4(vf[2][1], vv1, rWS, sv_ + 32768, 0); SCHED();
  MA32(aq[3], wq1, xq1[3]); SCHED();
  xq0[3] = LDS128(ax0[0] + 12288); SCHED();
  MA16(acs, cmask, wq0); MA32(aq[0], wq0, xq0[0]); SCHED();
  wq1 = TRF(aw0[0] + 18432, 512);
  xq1[0] = LDS128(ax1[0] + 0);
  BLOAD4(kf[3][0], vk0, rWS, sk + 57344, 0); SCHED();
  MA32(aq[1], wq0, xq0[1]); SCHED();
  xq1[1] = LDS128(ax1[0] + 4096); SCHED();
  MA32(aq[2], wq0, xq0[2]); SCHED();
  xq1[2] = LDS128(ax1[0] + 8192);
  BLOAD4(kf[3][1], vk1, rWS, sk + 57344, 0); SCHED();
  MA32(aq[3], wq0, xq0[3]); SCHED();
  xq1[3] = LDS128(ax1[0] + 12288); SCHED();
  MA16(acs, cmask, wq1); MA32(aq[0], wq1, xq1[0]); SCHED();
  wq0 = TRF(aw0[0] + 20480, 512);
  xq0[0] = LDS128(ax2[0] + 0);
  BLOAD4(kf[4][0], vk0, rWS, sk + 86016, 0); SCHED();
  MA32(aq[1], wq1, xq1[1]); SCHED();
  xq0[1] = LDS128(ax2[0] + 4096); SCHED();
  MA32(aq[2], wq1, xq1[2]); SCHED();
  xq0[2] = LDS128(ax2[0] + 8192);
  BLOAD4(kf[4][1], vk1, rWS, sk + 86016, 0); SCHED();
  MA32(aq[3], wq1, xq1[3]); SCHED();
  xq0[3] = LDS128(ax2[0] + 12288); SCHED();
  MA16(acs, cmask, wq0); MA32(aq[0], wq0, xq0[0]); SCHED();
  wq1 = TRF(aw0[0] + 22528, 512);
  xq1[0] = LDS128(ax3[0] + 0);
  xq1[1] = LDS128(ax3[0] + 4096);
  BLOAD4(vf[3][0], vv0, rWS, sv_ + 65536, 0); SCHED();
  MA32(aq[1], wq0, xq0[1]); SCHED();
  xq1[2] = LDS128(ax3[0] + 8192);
  xq1[3] = LDS128(ax3[0] + 12288); SCHED();
  MA32(aq[2], wq0, xq0[2]); SCHED();
  BLOAD4(vf[3][1], vv1, rWS, sv_ + 65536, 0); SCHED();
  MA32(aq[3], wq0, xq0[3]); SCHED();
  acs = MFMA16(cmask, wq1, acs); asm volatile("" : "+v"(acs)); MA32(aq[0], wq1, xq1[0]); SCHED();
  BLOAD4(vf[4][0], vv0, rWS, sv_ + 98304, 0); SCHED();
  MA32(aq[1], wq1, xq1[1]); SCHED();
  MA32(aq[2], wq1, xq1[2]); SCHED();
  BLOAD4(vf[4][1], vv1, rWS, sv_ + 98304, 0); SCHED();
  MA32(aq[3], wq1, xq1[3]); SCHED();
  ATT_QCORR(); SCHED();
  ATT_QEPI(0); SCHED();
  ATT_QEPI(1); SCHED();
  ATT_QEPI(2); SCHED();
  ATT_QEPI(3); SCHED();
  asm volatile("s_waitcnt vmcnt(14)" : "+v"(kf[0][0]), "+v"(kf[0][1]), "+v"(kf[1][0]), "+v"(kf[1][1]), "+v"(kf[2][0]), "+v"(kf[2][1]) :: "memory");
  ATT_SM(0);
  asm volatile("s_waitcnt vmcnt(8)" : "+v"(vf[0][0]), "+v"(vf[0][1]), "+v"(vf[1][0]), "+v"(vf[1][1]), "+v"(vf[2][0]), "+v"(vf[2][1]) :: "memory");
  ATT_PV(0);
  asm volatile("s_waitcnt vmcnt(4)" : "+v"(kf[3][0]), "+v"(kf[3][1]), "+v"(kf[4][0]), "+v"(kf[4][1]) :: "memory");
  ATT_OST(0);
  ATT_SM(1);
  asm volatile("s_waitcnt vmcnt(4)" : "+v"(vf[3][0]), "+v"(vf[3][1]), "+v"(vf[4][0]), "+v"(vf[4][1]) :: "memory");
  ATT_PV(1);
  ATT_OST(1);
  asm volatile("s_waitcnt vmcnt(0)" ::: "memory");
  FLAG(vfo);
    if (blockIdx.x == 0 && w == 0) {
      _Pragma("clang loop unroll(disable)") for (int k = 0; k < 4; ++k) {
        const u32 n = 64 * k + lane;
        const u32 vo_ = n < 224 ? WS_FL + n * 128 + 8 : WS_FO + (n - 224) * 1024;
        u32 t = ~ep;
        POLL_CHECK(t, vo_, 0);
      }
      FLAG(WS_EPOCH);
    }
  }
}

extern "C" void kernel_launch(void* const* d_in, const int* in_sizes, int n_in, void* d_out, int out_size, void* d_ws, size_t ws_size,
                              hipStream_t stream) {
  fused_kernel<<<256, 512, 0, stream>>>(d_in[0], d_in[1], d_in[2], d_in[4], d_in[5], d_in[6], d_out, d_ws);
}
```
